```python
import torch

def _softmax(x, axis=-1):
    return torch.softmax(x, dim=axis)
def _gelu(x):
    return torch.nn.functional.gelu(x, approximate="tanh")
def _silu(x):
    return torch.nn.functional.silu(x)
def _sigmoid(x):
    return torch.sigmoid(x)
def _layernorm(x, axis=-1, eps=1e-5):
    m = x.mean(dim=axis, keepdim=True)
    v = x.var(dim=axis, keepdim=True, unbiased=False)
    return (x - m) / torch.sqrt(v + eps)
def _scatter_add(tgt, idx, src, axis=0):
    return tgt.clone().index_add_(axis, idx, src)
def _segment_sum(src, seg, n):
    out = torch.zeros((n,) + src.shape[1:], dtype=src.dtype)
    return out.index_add_(0, seg, src)
def _pad(x, pw):
    flat = tuple(v for pair in reversed(pw) for v in pair)
    return torch.nn.functional.pad(x, flat)

kernel_name = 'recurrent_scan__linear_kernel_hedgehog__mixer_channel_mlp__prompt'

def setup_inputs():

    _w = (lambda a: (lambda t: t.to(torch.bfloat16) if t.is_floating_point() else t)(torch.as_tensor(a)))
    def _A_setup():
        B, S, D, H = 1, 2048, 512, 32
        Dh = D // H
        return {
            "x": _w((lambda *s: torch.randn(*s, dtype=torch.bfloat16) * 0.05)(B, S, D)),
            "wqkv": _w((lambda *s: torch.randn(*s, dtype=torch.bfloat16) * 0.2)(D, 3 * D)),
            "w_phi": _w((lambda *s: torch.randn(*s, dtype=torch.bfloat16) * 0.2)(H, Dh, Dh) * (Dh ** -0.5)),
            "wo": _w((lambda *s: torch.randn(*s, dtype=torch.bfloat16) * 0.2)(D, D)),
        }
    _A_in = _A_setup()

    def _B_setup():
        B, S, D, F = 1, 2048, 512, 1376
        return {
            "x": _w((lambda *s: torch.randn(*s, dtype=torch.bfloat16) * 0.05)(B, S, D)),
            "w1": _w((lambda *s: torch.randn(*s, dtype=torch.bfloat16) * 0.8)(D, F) * (D ** -0.5)),
            "b1": _w((lambda *s: torch.randn(*s, dtype=torch.bfloat16) * 0.8)(F)),
            "w2": _w((lambda *s: torch.randn(*s, dtype=torch.bfloat16) * 0.1)(F, D) * (F ** -0.5)),
            "b2": _w((lambda *s: torch.randn(*s, dtype=torch.bfloat16) * 0.1)(D)),
        }
    _B_in = _B_setup()

    return {
        "A_x": _A_in["x"],
        "A_wqkv": _A_in["wqkv"],
        "A_w_phi": _A_in["w_phi"],
        "A_wo": _A_in["wo"],
        "B_x": _B_in["x"],
        "B_w1": _B_in["w1"],
        "B_b1": _B_in["b1"],
        "B_w2": _B_in["w2"],
        "B_b2": _B_in["b2"],
    }

def reference(A_x, A_wqkv, A_w_phi, A_wo, B_x, B_w1, B_b1, B_w2, B_b2):
    def _A_ref(x, wqkv, w_phi, wo):
        B, S, D = x.shape
        H = 32; Dh = D // H
        qkv = (lambda x, *d: x.permute(*d))((x @ wqkv).reshape(B, S, 3, H, Dh), 2, 0, 3, 1, 4)
        q, k, v = qkv[0], qkv[1], qkv[2]
        pq = torch.einsum("bhsd,hdf->bhsf", q, w_phi)
        pk = torch.einsum("bhsd,hdf->bhsf", k, w_phi)
        phi_q = torch.concatenate([_softmax(pq, axis=-1), _softmax(-pq, axis=-1)], axis=-1)
        phi_k = torch.concatenate([_softmax(pk, axis=-1), _softmax(-pk, axis=-1)], axis=-1)
        kv = torch.einsum("bhsf,bhsd->bhfd", phi_k, v)
        z  = torch.einsum("bhsf->bhf", phi_k)
        num = torch.einsum("bhqf,bhfd->bhqd", phi_q, kv)
        den = torch.einsum("bhqf,bhf->bhq", phi_q, z)[..., None] + 1e-6
        o = num / den
        return (lambda x, *d: x.permute(*d))(o, 0, 2, 1, 3).reshape(B, S, D) @ wo

    def _B_ref(x, w1, b1, w2, b2):
        y = _layernorm(x, axis=-1)
        h = _gelu(torch.einsum("bsd,df->bsf", y, w1) + b1)
        return torch.einsum("bsf,fd->bsd", h, w2) + b2

    x = A_x
    xn = _layernorm(x, axis=-1)
    s = _A_ref(xn, A_wqkv, A_w_phi, A_wo)
    g = _silu(_B_ref(xn, B_w1, B_b1, B_w2, B_b2))
    return x + s * g
```

```cpp
#include <hip/hip_runtime.h>
#include <hip/hip_bf16.h>
typedef __hip_bfloat16 bf16;
constexpr int S=2048,D=512,H=32,DH=16,F=1376;
__device__ inline float b2f(bf16 v){return __bfloat162float(v);}
__device__ inline float rb(float v){return __bfloat162float(__float2bfloat16(v));}

__global__ void k_ln(const bf16* x, float* xn, float* y){
  int r=blockIdx.x; int t=threadIdx.x;
  float v[8]; float s=0;
  for(int j=0;j<8;j++){v[j]=b2f(x[r*D+t*8+j]); s+=v[j];}
  for(int o=32;o;o>>=1) s+=__shfl_xor(s,o);
  float m=s/D; float q=0;
  for(int j=0;j<8;j++){float d=v[j]-m;q+=d*d;}
  for(int o=32;o;o>>=1) q+=__shfl_xor(q,o);
  float var=q/D; float rs=1.f/sqrtf(var+1e-5f);
  float rs2=1.f/sqrtf(var*rs*rs+1e-5f);
  for(int j=0;j<8;j++){xn[r*D+t*8+j]=(v[j]-m)*rs; y[r*D+t*8+j]=(v[j]-m)*rs*rs2;}
}
__global__ void k_gemm(const float* A,const bf16* B,float* C,int N,int K){
  int n=blockIdx.x*64+threadIdx.x; int m=blockIdx.y;
  if(n>=N) return;
  float a=0; for(int k=0;k<K;k++) a+=A[m*K+k]*b2f(B[k*N+n]);
  C[m*N+n]=a;
}
__global__ void k_phi(const float* qkv,const bf16* wphi,float* phiq,float* phik){
  int i=blockIdx.x*256+threadIdx.x; int w=i&1; int h=(i>>1)&31; int s=i>>6;
  float p[16];
  for(int f=0;f<16;f++){float a=0; for(int d=0;d<16;d++) a+=rb(qkv[s*1536+w*512+h*16+d])*b2f(wphi[h*256+d*16+f]); p[f]=a;}
  float mx=p[0],mn=p[0]; for(int f=1;f<16;f++){mx=fmaxf(mx,p[f]);mn=fminf(mn,p[f]);}
  float sp=0,sn=0; for(int f=0;f<16;f++){sp+=__expf(p[f]-mx);sn+=__expf(mn-p[f]);}
  float* o=(w?phik:phiq)+(s*32+h)*32;
  for(int f=0;f<16;f++){o[f]=rb(__expf(p[f]-mx)/sp);o[16+f]=rb(__expf(mn-p[f])/sn);}
}
__global__ void k_kv(const float* qkv,const float* phik,float* kv){
  int h=blockIdx.x,t=threadIdx.x; int f=t/17,d=t%17; float a=0;
  for(int s=0;s<S;s++){float v=d<16?rb(qkv[s*1536+1024+h*16+d]):1.f; a+=phik[(s*32+h)*32+f]*v;}
  kv[h*544+t]=rb(a);
}
__global__ void k_o(const float* phiq,const float* kv,float* o){
  int i=blockIdx.x*256+threadIdx.x; int d=i&15,h=(i>>4)&31,s=i>>9;
  float n=0,dn=0; for(int f=0;f<32;f++){float p=phiq[(s*32+h)*32+f]; n+=p*kv[h*544+f*17+d]; dn+=p*kv[h*544+f*17+16];}
  o[i]=rb(n/(dn+1e-6f));
}
__global__ void k_gelu(float* hh,const bf16* b1){
  int i=blockIdx.x*256+threadIdx.x; int f=i%F; float v=hh[i]+b2f(b1[f]);
  float u=0.7978845608f*(v+0.044715f*v*v*v); hh[i]=rb(0.5f*v*(1.f+tanhf(u)));
}
__global__ void k_fin(const bf16* x,const float* s,const float* m,const bf16* b2,bf16* out){
  int i=blockIdx.x*256+threadIdx.x; int d=i%D; float mm=m[i]+b2f(b2[d]); float g=mm/(1.f+__expf(-mm));
  out[i]=__float2bfloat16(b2f(x[i])+s[i]*g);
}
extern "C" void kernel_launch(void* const* d_in, const int* in_sizes, int n_in, void* d_out, int out_size, void* d_ws, size_t ws_size, hipStream_t stream) {
  const bf16* x=(const bf16*)d_in[0]; const bf16* wqkv=(const bf16*)d_in[1]; const bf16* wphi=(const bf16*)d_in[2]; const bf16* wo=(const bf16*)d_in[3];
  const bf16* w1=(const bf16*)d_in[5]; const bf16* b1=(const bf16*)d_in[6]; const bf16* w2=(const bf16*)d_in[7]; const bf16* b2=(const bf16*)d_in[8];
  float* ws=(float*)d_ws;
  float* xn=ws; float* y=xn+S*D; float* qkv=y+S*D; float* phiq=qkv+S*1536; float* phik=phiq+S*1024; float* kv=phik+S*1024; float* o=kv+32*544+256; float* s=o+S*D; float* hh=s+S*D; float* m=hh+S*F;
  k_ln<<<S,64,0,stream>>>(x,xn,y);
  k_gemm<<<dim3(1536/64,S),64,0,stream>>>(xn,wqkv,qkv,1536,512);
  k_phi<<<S*64/256,256,0,stream>>>(qkv,wphi,phiq,phik);
  k_kv<<<32,544,0,stream>>>(qkv,phik,kv);
  k_o<<<S*D/256,256,0,stream>>>(phiq,kv,o);
  k_gemm<<<dim3(512/64,S),64,0,stream>>>(o,wo,s,512,512);
  k_gemm<<<dim3((F+63)/64,S),64,0,stream>>>(y,w1,hh,F,512);
  k_gelu<<<S*F/256,256,0,stream>>>(hh,b1);
  k_gemm<<<dim3(512/64,S),64,0,stream>>>(hh,w2,m,512,F);
  k_fin<<<S*D/256,256,0,stream>>>(x,s,m,b2,(bf16*)d_out);
}
```

```cpp
#include <hip/hip_runtime.h>
#include <hip/hip_bf16.h>

typedef unsigned u32;
typedef unsigned long long u64;
typedef __bf16 bf16x2 __attribute__((ext_vector_type(2)));
typedef __bf16 bf16x8 __attribute__((ext_vector_type(8)));
typedef short s16x4 __attribute__((ext_vector_type(4)));
typedef float f32x2 __attribute__((ext_vector_type(2)));
typedef float f32x4 __attribute__((ext_vector_type(4)));
typedef float f32x16 __attribute__((ext_vector_type(16)));
typedef u32 u32x2 __attribute__((ext_vector_type(2)));
typedef u32 u32x4 __attribute__((ext_vector_type(4)));
#define LAS __attribute__((address_space(3)))
#define GAS __attribute__((address_space(1)))
#define DI __device__ __forceinline__

#define PROF 0
#define MAPMODE 0
#define ROTMODE 1
#define CODEPF 1

constexpr u32 WS_FP1 = 4096;
constexpr u32 WS_FH = WS_FP1 + 512 * 128;
constexpr u32 WS_FO = WS_FH + 128 * 128;
constexpr u32 WS_PZ = WS_FO + 128 * 128;
constexpr u32 WS_PKV = WS_PZ + 128 * 256;
constexpr u32 WS_O = WS_PKV + 128 * 4096;
constexpr u32 WS_H = WS_O + 16 * 64 * 2048;
constexpr u32 WS_END = WS_H + 16 * 172 * 2048;
constexpr u32 WS_PROF = 16u << 20;
constexpr u32 OOB = 0x7ff00000u;

constexpr u32 SLOT = 40960;
constexpr u32 L_PHI = 40960;
constexpr u32 L_V = L_PHI + 32768;
constexpr u32 L_SMALLW = 18 * 8192;
constexpr u32 L_SCR = 19 * 8192;
constexpr u32 L_XST = L_SCR;
constexpr u32 L_CS = L_SCR + 4096;
constexpr u32 L_RS = L_SCR + 7168;
constexpr u32 L_KV = L_SCR;
constexpr u32 L_Z = L_SCR + 4096;
constexpr u32 L_XCH = 11 * 8192;

constexpr float LOG2E = 1.4426950408889634f;

DI u32x4 mk_srd(const void* p, u32 bytes) {
    u64 a = (u64)p;
    u32x4 r = {(u32)a, (u32)(a >> 32) & 0xffffu, bytes, 0x00020000u};
    return r;
}
DI void dma16(u32 lds, u32 voff, u32x4 srd, u32 soff) {
    asm volatile("s_mov_b32 m0, %0\n\ts_nop 0\n\tbuffer_load_dwordx4 %1, %2, %3 offen lds"
                 :: "s"(lds), "v"(voff), "s"(srd), "s"(soff) : "memory");
}
#define BARRIER() asm volatile("s_waitcnt lgkmcnt(0)\n\ts_barrier" ::: "memory")
#define WAITV_BARRIER(n) asm volatile("s_waitcnt vmcnt(" #n ") lgkmcnt(0)\n\ts_barrier" ::: "memory")
#define WAITV(n) asm volatile("s_waitcnt vmcnt(" #n ")" ::: "memory")

DI u32 pk2(float a, float b) {
    f32x2 f = {a, b};
    return __builtin_bit_cast(u32, __builtin_convertvector(f, bf16x2));
}
DI bf16x8 asfrag(u32x4 v) { return __builtin_bit_cast(bf16x8, v); }
DI float addf(float a, float b) { float r; asm("v_add_f32 %0, %1, %2" : "=v"(r) : "v"(a), "v"(b)); return r; }
DI float fmacf(float acc, float a) { asm("v_fmac_f32 %0, %1, %1" : "+v"(acc) : "v"(a)); return acc; }
#define MFMA32(a, b, c) __builtin_amdgcn_mfma_f32_32x32x16_bf16((a), (b), (c), 0, 0, 0)
#define MFMA16(a, b, c) __builtin_amdgcn_mfma_f32_16x16x32_bf16((a), (b), (c), 0, 0, 0)

struct Lds {
    LAS char* p;
    DI u32x4 r128(u32 a) const { return *(LAS u32x4*)(p + a); }
    DI f32x4 r128f(u32 a) const { return *(LAS f32x4*)(p + a); }
    DI void w128(u32 a, u32x4 v) const { *(LAS u32x4*)(p + a) = v; }
    DI void w64(u32 a, u32x2 v) const { *(LAS u32x2*)(p + a) = v; }
    DI void w32f(u32 a, float v) const { *(LAS float*)(p + a) = v; }
    DI u32 r16(u32 a) const { return *(LAS unsigned short*)(p + a); }
    DI bf16x8 tr(u32 a, u32 d) const {
        s16x4 lo = __builtin_amdgcn_ds_read_tr16_b64_v4i16((LAS s16x4*)(p + a));
        s16x4 hi = __builtin_amdgcn_ds_read_tr16_b64_v4i16((LAS s16x4*)(p + a + d));
        return __builtin_bit_cast(bf16x8, __builtin_shufflevector(lo, hi, 0, 1, 2, 3, 4, 5, 6, 7));
    }
};

DI void poll_flags(const char* ws, u32 off, bool active, u64 tag) {
    GAS u64* f = (GAS u64*)(ws + off);
    for (u32 spins = 0; spins < (1u << 18); ++spins) {
        u64 v = tag;
        if (active) v = __hip_atomic_load(f, __ATOMIC_RELAXED, __HIP_MEMORY_SCOPE_AGENT);
        if (__all(v == tag)) break;
        __builtin_amdgcn_s_sleep(1);
    }
    asm volatile("" ::: "memory");
}
DI void set_flag(char* ws, u32 off, u64 tag) {
    __hip_atomic_store((GAS u64*)(ws + off), tag, __ATOMIC_RELAXED, __HIP_MEMORY_SCOPE_AGENT);
}

DI void dual_softmax(const f32x16& p, float sc, u32 (&o)[16]) {
    float mx = p[0], mn = p[0];
#pragma unroll
    for (int i = 1; i < 16; ++i) { mx = __builtin_elementwise_maximum(mx, p[i]); mn = __builtin_elementwise_minimum(mn, p[i]); }
    const float c = sc * LOG2E;
    const f32x2 cp = {c, c}, cn = {-c, -c};
    const float bx = -mx * c, bn = mn * c;
    const f32x2 bxp = {bx, bx}, bnp = {bn, bn};
    f32x2 ep[8], en[8];
    f32x2 sp = {0.f, 0.f}, sn = {0.f, 0.f};
#pragma unroll
    for (int i = 0; i < 8; ++i) {
        const f32x2 v = {p[2 * i], p[2 * i + 1]};
        const f32x2 a = __builtin_elementwise_fma(v, cp, bxp), b = __builtin_elementwise_fma(v, cn, bnp);
        ep[i] = f32x2{__builtin_amdgcn_exp2f(a[0]), __builtin_amdgcn_exp2f(a[1])};
        en[i] = f32x2{__builtin_amdgcn_exp2f(b[0]), __builtin_amdgcn_exp2f(b[1])};
        sp += ep[i]; sn += en[i];
    }
    const float rp = __builtin_amdgcn_rcpf(sp[0] + sp[1]), rn = __builtin_amdgcn_rcpf(sn[0] + sn[1]);
    const f32x2 rpp = {rp, rp}, rnp = {rn, rn};
#pragma unroll
    for (int i = 0; i < 8; ++i) {
        const f32x2 a = ep[i] * rpp, b = en[i] * rnp;
        o[i] = pk2(a[0], a[1]); o[8 + i] = pk2(b[0], b[1]);
    }
}
DI bf16x8 pack8(const f32x16& a, int s) {
    u32x4 r = {pk2(a[8 * s], a[8 * s + 1]), pk2(a[8 * s + 2], a[8 * s + 3]), pk2(a[8 * s + 4], a[8 * s + 5]), pk2(a[8 * s + 6], a[8 * s + 7])};
    return asfrag(r);
}

#define STAMP(i) do {} while (0)

template <bool QKV>
DI void gemm_a(const Lds L, u32 w, u32 l, u32 k0, u32x4 srdx, u32x4 srdw, u32x4 srd2, u32 wtile, const u32 (&vx)[2], const u32 (&vw)[3],
               u32 v2, u32 s2base, f32x16 (&acc)[3], f32x4 (&cs)[3], float& xs, float& xq) {
    const u32 tg = w & 3, hp = w >> 2, r = l & 31, h = l >> 5;
    const u32 rot = tg ^ (2 * hp);
    const u32 q = (l & 15) >> 2, p = l & 3, blk = (l >> 4) & 1, ps = ((p & 1) << 1) | (p >> 1);
    u32 xa[4], wa[4];
#pragma unroll
    for (int i = 0; i < 4; ++i) {
        const u32 s = i ^ rot;
        xa[i] = (32 * tg + r) * 128 + 16 * ((2 * s + h) ^ ((r >> 1) & 7));
        wa[i] = 16384 + (16 * s + 8 * h + q) * 384 + 64 * (hp ^ (q >> 1)) + 32 * blk + 8 * ps;
    }
    const u32 one = ((l & 3) == 0 && ((l >> 2) & 1) == ((l >> 4) & 1)) ? 0x3f803f80u : 0u;
    const bf16x8 sel = asfrag(u32x4{one, one, one, one});
    float s0 = 0.f, s1 = 0.f, q0 = 0.f, q1 = 0.f;
    u32 cur = 0, nxt = 2 * SLOT;
#pragma unroll 1
    for (u32 t = 0; t < 8; ++t) {
        WAITV_BARRIER(5);
        if (t < 6) {
            const u32 kt = (k0 + t + 2) & 7;
            dma16(nxt + w * 2048, vx[0], srdx, kt * 128);
            dma16(nxt + w * 2048 + 1024, vx[1], srdx, kt * 128);
#pragma unroll
            for (int j = 0; j < 3; ++j) dma16(nxt + 16384 + w * 3072 + j * 1024, vw[j], srdw, kt * wtile);
        } else if (t >= 6) {
            const u32 r0 = (t == 6) ? 11 : 0;
#pragma unroll
            for (int j = 0; j < 5; ++j) dma16((r0 + j) * 8192 + w * 1024, v2, srd2, s2base + (r0 + j) * 32768);
        }
#pragma unroll
        for (int i = 0; i < 4; ++i) {
            const u32x4 xr = L.r128(cur + xa[i]);
            const bf16x8 xf = asfrag(xr);
            const bf16x8 w0 = L.tr(cur + wa[i], 4 * 384), w1 = L.tr(cur + wa[i] + 128, 4 * 384), w2 = L.tr(cur + wa[i] + 256, 4 * 384);
            acc[0] = MFMA32(w0, xf, acc[0]);
            acc[1] = MFMA32(w1, xf, acc[1]);
            acc[2] = QKV ? MFMA32(xf, w2, acc[2]) : MFMA32(w2, xf, acc[2]);
            if (i < 2) {
#pragma unroll
                for (int d = 0; d < 4; ++d) {
                    const float lo = __builtin_bit_cast(float, xr[d] << 16), hi = __builtin_bit_cast(float, xr[d] & 0xffff0000u);
                    s0 = addf(s0, lo); s1 = addf(s1, hi); q0 = fmacf(q0, lo); q1 = fmacf(q1, hi);
                }
            }
            if (i == 2) { cs[0] = MFMA16(sel, w0, cs[0]); cs[1] = MFMA16(sel, w1, cs[1]); cs[2] = MFMA16(sel, w2, cs[2]); }
        }
        nxt = cur; cur = (cur == 2 * SLOT) ? 0 : cur + SLOT;
    }
    xs = s0 + s1; xq = q0 + q1;
}

__global__ void __launch_bounds__(512, 2)
hedgehog_mixer_kernel(const char* __restrict__ x, const char* __restrict__ wqkv, const char* __restrict__ wphi, const char* __restrict__ wo,
                      const char* __restrict__ w1, const char* __restrict__ b1, const char* __restrict__ w2, const char* __restrict__ b2,
                      char* __restrict__ out, char* ws) {
    __shared__ __attribute__((aligned(16))) char smem[163840];
    const Lds L{(LAS char*)smem};
    STAMP(0);
    const u32 b = blockIdx.x, xcd = b & 7, bi = b >> 3;
    const u32 l = threadIdx.x & 63, w = __builtin_amdgcn_readfirstlane(threadIdx.x >> 6);
    {
        u64 pc; u32 len;
        asm volatile("s_getpc_b64 %0\n.Lpf_here:\n\ts_mov_b32 %1, .Lpf_end-.Lpf_here" : "=s"(pc), "=s"(len));
        if (w == 7) {
            const u32x4 sc = mk_srd((const void*)pc, len);
            u32 dummy;
            const u32 off = (l < 8) ? (l * 32 + bi) * 128 : OOB;
            asm volatile("s_nop 4\n\tbuffer_load_dword %0, %1, %2, 0 offen\n\ts_waitcnt vmcnt(0)" : "=&v"(dummy) : "v"(off), "s"(sc) : "memory");
        }
    }
    const u32 rt = (xcd & 1) * 8 + (bi & 7), isq = (bi >> 3) & 1, ci = (xcd >> 1) * 2 + (bi >> 4);
    const u32 k0 = (rt + 2 * ci + isq) & 7;
    const u32 rb = xcd * 4 + (bi & 3), cb = bi >> 2;
    const u32 u0 = ci * 5 + (ci < 3 ? ci : 3), nu = ci < 3 ? 6 : 5;
    const u64 tag = *(const __attribute__((address_space(4))) u64*)(u64)ws + 1;

    const u32x4 srdx = mk_srd(x + rt * 131072, 131072);
    const u32x4 srdw = isq ? mk_srd(wqkv, 512 * 3072) : mk_srd(w1, 512 * 2752);
    const u32 wrow = isq ? 3072 : 2752;
    u32 vx[2], vw[3];
#pragma unroll
    for (int j = 0; j < 2; ++j) vx[j] = (16 * w + 8 * j + (l >> 3)) * 1024 + 16 * ((l & 7) ^ (l >> 4) ^ (4 * j));
#pragma unroll
    for (int j = 0; j < 3; ++j) {
        const u32 o = 64 * j + l, row = o / 24, pos = o % 24, bs = (pos >> 2) ^ ((row >> 1) & 1);
        const u32 col = isq ? (bs >> 1) * 1024 + ci * 128 + (bs & 1) * 64 : (u0 + bs) * 64;
        vw[j] = (!isq && bs >= nu) ? OOB : (8 * w + row) * wrow + col + 16 * (pos & 3);
    }
#pragma unroll
    for (int t = 0; t < 2; ++t) {
        const u32 kt = (k0 + t) & 7;
        dma16(t * SLOT + w * 2048, vx[0], srdx, kt * 128);
        dma16(t * SLOT + w * 2048 + 1024, vx[1], srdx, kt * 128);
#pragma unroll
        for (int j = 0; j < 3; ++j) dma16(t * SLOT + 16384 + w * 3072 + j * 1024, vw[j], srdw, kt * 64 * wrow);
    }
    {
        const u32x4 srds = isq ? mk_srd(wphi + ci * 2048, 2048) : mk_srd(b1 + u0 * 64, nu * 64);
        dma16(L_SMALLW + w * 1024, w * 1024 + l * 16, srds, 0);
    }
    const u32 kh = w >> 2, tq = w & 3;
    const u32x4 srd2 = mk_srd(w2, 1376 * 1024), srdo = mk_srd(wo, 512 * 1024);
    const u32 v2 = (8 * tq + (l >> 3)) * 1024 + cb * 128 + 16 * ((l & 7) ^ (2 * (((l >> 4) & 1) | ((w & 1) << 1))));
    const u32 s2base = kh * 22 * 32768;

    const u32 tg = w & 3, hp = w >> 2, r = l & 31, h = l >> 5;
    f32x16 acc[3]; f32x4 cs[3];
#pragma unroll
    for (int j = 0; j < 3; ++j) { acc[j] = f32x16{}; cs[j] = f32x4{}; }
    float xs, xq;
    STAMP(1);
    if (isq) gemm_a<true>(L, w, l, k0, srdx, srdw, srd2, 64 * 3072, vx, vw, v2, s2base, acc, cs, xs, xq);
    else gemm_a<false>(L, w, l, k0, srdx, srdw, srd2, 64 * 2752, vx, vw, v2, s2base, acc, cs, xs, xq);
    STAMP(2);

    const u32 p4 = (l >> 2) & 3, ps4 = ((p4 & 1) << 1) | (p4 >> 1);
    const u32 feat = (l & 16) + 4 * ps4 + (l & 3);
    L.w64(L_XST + ((tg * 32 + r) * 4 + hp * 2 + h) * 8, u32x2{__builtin_bit_cast(u32, xs), __builtin_bit_cast(u32, xq)});
    if (l < 32) {
#pragma unroll
        for (int j = 0; j < 3; ++j) L.w32f(L_CS + ((hp * 3 + j) * 32 + l) * 16 + tg * 4, cs[j][0]);
    }
    u32 bias[3] = {0, 0, 0};
    bf16x8 wph = {};
    if (isq) {
        const u32 q = (l & 15) >> 2, p = l & 3, blk = (l >> 4) & 1;
        wph = L.tr(L_SMALLW + (2 * hp + (p & 1)) * 512 + (8 * h + q) * 32 + (2 * blk + (p >> 1)) * 8, 128);
    } else {
#pragma unroll
        for (int j = 0; j < 3; ++j) bias[j] = L.r16(L_SMALLW + ((2 * j + hp) * 32 + feat) * 2);
    }
    BARRIER();
#pragma unroll
    for (int j = 0; j < 3; ++j) dma16((16 + j) * 8192 + w * 1024, v2, srd2, s2base + (16 + j) * 32768);
    float rs;
    {
        const f32x4 a = L.r128f(L_XST + (tg * 32 + r) * 32), c = L.r128f(L_XST + (tg * 32 + r) * 32 + 16);
        const float mean = ((a[0] + a[2]) + (c[0] + c[2])) * (1.f / 512.f);
        const float var = fmaxf(((a[1] + a[3]) + (c[1] + c[3])) * (1.f / 512.f) - mean * mean, 0.f);
        rs = __builtin_amdgcn_rsqf(var + 1e-5f);
        if (!isq) rs *= __builtin_amdgcn_rsqf(var * rs * rs + 1e-5f);
        const float sd = __builtin_amdgcn_rcpf(rs);
        const u32 mh = pk2(-mean, 0.f) & 0xffffu, sh = pk2(sd, 0.f) & 0xffffu;
        const u32 ml = pk2(-mean - __builtin_bit_cast(float, mh << 16), 0.f) & 0xffffu;
        const u32 sl = pk2(sd - __builtin_bit_cast(float, sh << 16), 0.f) & 0xffffu;
        const bf16x8 xfix = asfrag(u32x4{mh | (ml << 16), mh | (sh << 16), sl, 0u});
#pragma unroll
        for (int j = 0; j < 3; ++j) {
            const f32x4 cp = L.r128f(L_CS + ((hp * 3 + j) * 32 + r) * 16);
            const float c0 = (cp[0] + cp[1]) + (cp[2] + cp[3]);
            const u32 ch = pk2(c0, 0.f) & 0xffffu, cl = pk2(c0 - __builtin_bit_cast(float, ch << 16), 0.f) & 0xffffu;
            u32x4 wf = {ch | (ch << 16), cl | (bias[j] << 16), bias[j], 0u};
            if (h) wf = u32x4{0u, 0u, 0u, 0u};
            if (j == 2 && isq) acc[j] = MFMA32(xfix, asfrag(wf), acc[j]);
            else acc[j] = MFMA32(asfrag(wf), xfix, acc[j]);
        }
    }
    const auto rws = __builtin_amdgcn_make_buffer_rsrc(ws, 0, WS_END, 0x00020000);
    const auto r2 = __builtin_amdgcn_make_buffer_rsrc((void*)w2, 0, 1376 * 1024, 0x00020000);
    const auto ro = __builtin_amdgcn_make_buffer_rsrc((void*)wo, 0, 512 * 1024, 0x00020000);
    u32x4 raw[11];

    if (!isq) {
        const float ga = -2.f * 0.7978845608028654f * LOG2E * rs, gb = ga * 0.044715f * rs * rs, sdv = __builtin_amdgcn_rcpf(rs);
        const f32x2 gap = {ga, ga}, gbp = {gb, gb}, sdp = {sdv, sdv};
#pragma unroll
        for (int j = 0; j < 3; ++j) {
            dma16((5 + 2 * j) * 8192 + w * 1024, v2, srd2, s2base + (5 + 2 * j) * 32768);
            dma16((6 + 2 * j) * 8192 + w * 1024, v2, srd2, s2base + (6 + 2 * j) * 32768);
            if (2 * j + hp < nu) {
                u32 o[8];
#pragma unroll
                for (int i = 0; i < 8; ++i) {
                    const f32x2 v = {acc[j][2 * i], acc[j][2 * i + 1]};
                    const f32x2 z = v * __builtin_elementwise_fma(v * v, gbp, gap);
                    const f32x2 e = {__builtin_amdgcn_exp2f(z[0]), __builtin_amdgcn_exp2f(z[1])};
                    const f32x2 d = __builtin_elementwise_fma(e, sdp, sdp);
                    const f32x2 y = v * f32x2{__builtin_amdgcn_rcpf(d[0]), __builtin_amdgcn_rcpf(d[1])};
                    o[i] = pk2(y[0], y[1]);
                }
                const u32 ho = WS_H + ((rt * 172 + 4 * (u0 + 2 * j + hp) + h) * 128 + 32 * tg + r) * 16;
                __builtin_amdgcn_raw_buffer_store_b128(u32x4{o[0], o[1], o[2], o[3]}, rws, ho, 0, 16);
                __builtin_amdgcn_raw_buffer_store_b128(u32x4{o[4], o[5], o[6], o[7]}, rws, ho + 4096, 0, 16);
            }
        }
        STAMP(3);
        WAITV_BARRIER(0);
        if (threadIdx.x == 64) set_flag(ws, WS_FH + (rt * 8 + ci) * 128, tag);
        STAMP(4);
#pragma unroll
        for (int j = 0; j < 3; ++j) raw[j] = __builtin_amdgcn_raw_buffer_load_b128(r2, v2, s2base + (19 + j) * 32768, 0);
#pragma unroll
        for (int j = 0; j < 8; ++j) raw[3 + j] = __builtin_amdgcn_raw_buffer_load_b128(ro, v2, (kh * 8 + j) * 32768, 0);
    } else {
        const u32 hm = ((l >> 2) & 1);
        const u32x4 wpu = __builtin_bit_cast(u32x4, wph);
        const u32 m0 = hm ? 0u : ~0u, m1 = ~m0;
        const bf16x8 wp0 = asfrag(wpu & m0), wp1 = asfrag(wpu & m1);
        if (l < 32) L.w32f(L_RS + w * 128 + l * 4, rs);
        {
            f32x16 pk = MFMA32(wp0, pack8(acc[1], 0), f32x16{});
            pk = MFMA32(wp1, pack8(acc[1], 1), pk);
            u32 vp[8];
#pragma unroll
            for (int g = 0; g < 4; ++g) {
                const f32x4 rr = L.r128f(L_RS + w * 128 + (8 * g + 4 * h) * 4);
                vp[2 * g] = pk2(acc[2][4 * g] * rr[0], acc[2][4 * g + 1] * rr[1]);
                vp[2 * g + 1] = pk2(acc[2][4 * g + 2] * rr[2], acc[2][4 * g + 3] * rr[3]);
            }
            L.w128(L_V + w * 2048 + l * 16, u32x4{vp[0], vp[1], vp[2], vp[3]});
            L.w128(L_V + w * 2048 + 1024 + l * 16, u32x4{vp[4], vp[5], vp[6], vp[7]});
            u32 ph[16];
            dual_softmax(pk, rs, ph);
            const u32 pa = L_PHI + ((2 * hp + h) * 128 + 32 * tg + r) * 64;
#pragma unroll
            for (int c = 0; c < 4; ++c) L.w128(pa + 16 * (c ^ ((r >> 1) & 3)), u32x4{ph[4 * c], ph[4 * c + 1], ph[4 * c + 2], ph[4 * c + 3]});
        }
        BARRIER();
        STAMP(3);
        if (w < 4) {
            const u32 q = (l & 15) >> 2, p = l & 3, blk = (l >> 4) & 1, ps = ((p & 1) << 1) | (p >> 1);
            const u32 tok = 4 * h + q;
            const u32 ch = 2 * blk + (ps >> 1);
            const u32 a0 = L_PHI + (w * 128 + tok) * 64 + 16 * (ch ^ ((tok >> 1) & 3)) + 8 * (ps & 1);
            const bool own = blk == (w & 1);
            f32x16 kv = {};
#pragma unroll
            for (int s = 0; s < 8; ++s) {
                const bf16x8 af = L.tr(a0 + s * 1024, 512);
                u32x4 vf = L.r128(L_V + (w >> 1) * 8192 + s * 1024 + l * 16);
                if (!own) vf = u32x4{0x3f803f80u, 0x3f803f80u, 0x3f803f80u, 0x3f803f80u};
                kv = MFMA32(af, asfrag(vf), kv);
            }
            const u32 pb = ci * 16 + rt;
            const u32 ok = WS_PKV + pb * 4096 + (w * 4 + h) * 256 + (feat & 15) * 16, oz = WS_PZ + pb * 256 + (w * 4 + h) * 16;
            const u32 o0 = own ? ok : ((l & 15) == 0 ? oz : OOB), o1 = own ? ok + 512 : ((l & 15) == 0 ? oz + 32 : OOB);
            __builtin_amdgcn_raw_buffer_store_b128(__builtin_bit_cast(u32x4, pack8(kv, 0)), rws, o0, 0, 16);
            __builtin_amdgcn_raw_buffer_store_b128(__builtin_bit_cast(u32x4, pack8(kv, 1)), rws, o1, 0, 16);
        }
        u32 pq[16];
        {
            f32x16 pp = MFMA32(wp0, pack8(acc[0], 0), f32x16{});
            pp = MFMA32(wp1, pack8(acc[0], 1), pp);
            dual_softmax(pp, rs, pq);
        }
        if (w < 4) {
            WAITV(0);
            if (l == 0) set_flag(ws, WS_FP1 + ((ci * 16 + rt) * 4 + w) * 128, tag);
        }
        STAMP(4);
        BARRIER();
#pragma unroll
        for (int j = 5; j < 11; ++j) dma16(j * 8192 + w * 1024, v2, srd2, s2base + j * 32768);
#pragma unroll
        for (int j = 0; j < 3; ++j) raw[j] = __builtin_amdgcn_raw_buffer_load_b128(r2, v2, s2base + (19 + j) * 32768, 0);
#pragma unroll
        for (int j = 0; j < 8; ++j) raw[3 + j] = __builtin_amdgcn_raw_buffer_load_b128(ro, v2, (kh * 8 + j) * 32768, 0);
        if (w == 0) poll_flags(ws, WS_FP1 + (ci * 64 + l) * 128, true, tag);
        BARRIER();
        STAMP(5);
        {
            u32x4 g[8];
            const u32 go = WS_PKV + (ci * 16 + 8 * h) * 4096 + (32 * w + r) * 16;
#pragma unroll
            for (int n = 0; n < 8; ++n) g[n] = __builtin_amdgcn_raw_buffer_load_b128(rws, go, n * 4096, 16);
            const u32x4 zr = __builtin_amdgcn_raw_buffer_load_b128(rws, WS_PZ + (ci * 16 + 2 * w) * 256 + r * 16, 0, 16);
            float sm[8];
#pragma unroll
            for (int j = 0; j < 8; ++j) sm[j] = 0.f;
#pragma unroll
            for (int n = 0; n < 8; ++n)
#pragma unroll
                for (int d = 0; d < 4; ++d) {
                    sm[2 * d] += __builtin_bit_cast(float, g[n][d] << 16);
                    sm[2 * d + 1] += __builtin_bit_cast(float, g[n][d] & 0xffff0000u);
                }
            float t4[4];
#pragma unroll
            for (int j = 0; j < 4; ++j) {
                const auto sw = __builtin_amdgcn_permlane32_swap(__builtin_bit_cast(u32, sm[j]), __builtin_bit_cast(u32, sm[j + 4]), false, false);
                t4[j] = __uint_as_float(sw[0]) + __uint_as_float(sw[1]);
            }
            L.w64(L_KV + (32 * w + r) * 16 + 8 * h, u32x2{pk2(t4[0], t4[1]), pk2(t4[2], t4[3])});
            if (l < 32) L.w128(L_Z + (l & 15) * 256 + (2 * w + (l >> 4)) * 16, zr);
        }
        BARRIER();
        STAMP(6);
        {
            const u32 fa = (2 * hp + h) * 1024 + ((r & 3) + 4 * (r >> 3)) * 16;
            const u32 am = (hm == h) ? ~0u : 0u;
            f32x16 num = {}, den = {};
#pragma unroll
            for (int s = 0; s < 4; ++s) {
                const bf16x8 bq = asfrag(u32x4{pq[4 * s], pq[4 * s + 1], pq[4 * s + 2], pq[4 * s + 3]});
                den = MFMA32(asfrag(L.r128(L_Z + fa + s * 256) & am), bq, den);
                num = MFMA32(asfrag(L.r128(L_KV + fa + s * 256) & am), bq, num);
            }
            f32x2 d2 = {den[0], den[1]};
#pragma unroll
            for (int i = 1; i < 8; ++i) d2 += f32x2{den[2 * i], den[2 * i + 1]};
            const float rd = __builtin_amdgcn_rcpf(d2[0] + d2[1] + 1e-6f);
            num *= rd;
            const u32 oo = WS_O + ((rt * 64 + 2 * (4 * ci + 2 * hp + h)) * 128 + 32 * tg + r) * 16;
            __builtin_amdgcn_raw_buffer_store_b128(__builtin_bit_cast(u32x4, pack8(num, 0)), rws, oo, 0, 16);
            __builtin_amdgcn_raw_buffer_store_b128(__builtin_bit_cast(u32x4, pack8(num, 1)), rws, oo + 2048, 0, 16);
        }
        WAITV_BARRIER(0);
        if (threadIdx.x == 64) set_flag(ws, WS_FO + (rt * 8 + ci) * 128, tag);
        STAMP(7);
    }

    const u32 prt = rb >> 1, g = l >> 4, tk = l & 15;
    const u32 tokl = 64 * (rb & 1) + 16 * tq + tk;
    u32x2 xres[2], bres[2];
#pragma unroll
    for (int j = 0; j < 2; ++j) {
        const u32 col = cb * 64 + 32 * kh + 16 * j + 4 * g;
        xres[j] = *(const u32x2*)(x + (size_t)(prt * 128 + tokl) * 1024 + col * 2);
        bres[j] = *(const u32x2*)(b2 + col * 2);
    }
    if (w == 0) poll_flags(ws, WS_FH + (prt * 8 + (l & 7)) * 128, l < 8, tag);
    WAITV_BARRIER(0);
    STAMP(8);
    u32x4 hf[22];
    {
        const u32 ho = WS_H + ((prt * 172 + 88 * kh + g) * 128 + tokl) * 16;
#pragma unroll
        for (int j = 0; j < 22; ++j) hf[j] = __builtin_amdgcn_raw_buffer_load_b128(rws, (j == 21 && kh) ? OOB : ho, j * 8192, 16);
    }
    u32 ba[4];
    {
        const u32 q = (l & 15) >> 2, p = l & 3, swz = (q >> 1) | ((g & 1) << 1);
#pragma unroll
        for (int c = 0; c < 4; ++c) ba[c] = kh * 4096 + (8 * g + q) * 128 + 32 * (c ^ swz) + 8 * p;
    }
    f32x4 am[4], as[4];
#pragma unroll
    for (int c = 0; c < 4; ++c) { am[c] = f32x4{}; as[c] = f32x4{}; }
#pragma unroll
    for (int j = 0; j < 22; ++j) {
        if (j == 11) {
            BARRIER();
#pragma unroll
            for (int n = 0; n < 11; ++n) L.w128(n * 8192 + w * 1024 + l * 16, raw[n]);
        }
        if (j == 19) BARRIER();
        const u32 sl = (j < 19 ? j : j - 19) * 8192;
#pragma unroll
        for (int c = 0; c < 4; ++c) am[c] = MFMA16(L.tr(sl + ba[c], 512), asfrag(hf[j]), am[c]);
    }
    STAMP(9);
    const u32 xo = L_XCH + w * 4096 + l * 16, xi = L_XCH + (w ^ 4) * 4096 + l * 16;
    L.w128(xo, __builtin_bit_cast(u32x4, kh ? am[0] : am[2]));
    L.w128(xo + 1024, __builtin_bit_cast(u32x4, kh ? am[1] : am[3]));
    if (w == 0) {
        poll_flags(ws, WS_FO + (prt * 8 + (l & 7)) * 128, l < 8, tag);
        if (b == 0) {
            poll_flags(ws, WS_FH + l * 128, true, tag);
            poll_flags(ws, WS_FH + (64 + l) * 128, true, tag);
            if (l == 0) set_flag(ws, 0, tag);
        }
    }
    BARRIER();
    STAMP(10);
    {
        u32x4 of[8];
        const u32 oo = WS_O + ((prt * 64 + 32 * kh + g) * 128 + tokl) * 16;
#pragma unroll
        for (int j = 0; j < 8; ++j) of[j] = __builtin_amdgcn_raw_buffer_load_b128(rws, oo, j * 8192, 16);
#pragma unroll
        for (int j = 0; j < 8; ++j)
#pragma unroll
            for (int c = 0; c < 4; ++c) as[c] = MFMA16(L.tr((3 + j) * 8192 + ba[c], 512), asfrag(of[j]), as[c]);
    }
    L.w128(xo + 2048, __builtin_bit_cast(u32x4, kh ? as[0] : as[2]));
    L.w128(xo + 3072, __builtin_bit_cast(u32x4, kh ? as[1] : as[3]));
    f32x4 gate[2];
#pragma unroll
    for (int j = 0; j < 2; ++j) {
        const f32x4 mo = L.r128f(xi + j * 1024), mm = kh ? am[2 + j] : am[j];
#pragma unroll
        for (int e = 0; e < 4; ++e) {
            const u32 bw = bres[j][e >> 1];
            const float m = mo[e] + mm[e] + __builtin_bit_cast(float, (e & 1) ? (bw & 0xffff0000u) : (bw << 16));
            gate[j][e] = m * __builtin_amdgcn_rcpf(1.f + __builtin_amdgcn_exp2f(-LOG2E * m));
        }
    }
    BARRIER();
    STAMP(11);
#pragma unroll
    for (int j = 0; j < 2; ++j) {
        const f32x4 so = L.r128f(xi + 2048 + j * 1024), sm = kh ? as[2 + j] : as[j];
        float y[4];
#pragma unroll
        for (int e = 0; e < 4; ++e) {
            const u32 xw = xres[j][e >> 1];
            y[e] = __builtin_bit_cast(float, (e & 1) ? (xw & 0xffff0000u) : (xw << 16)) + (so[e] + sm[e]) * gate[j][e];
        }
        const u32 col = cb * 64 + 32 * kh + 16 * j + 4 * g;
        *(u32x2*)(out + (size_t)(prt * 128 + tokl) * 1024 + col * 2) = u32x2{pk2(y[0], y[1]), pk2(y[2], y[3])};
    }
    asm volatile(".Lpf_end:");
}

extern "C" void kernel_launch(void* const* d_in, const int* in_sizes, int n_in, void* d_out, int out_size, void* d_ws, size_t ws_size,
                              hipStream_t stream) {
    hedgehog_mixer_kernel<<<256, 512, 0, stream>>>((const char*)d_in[0], (const char*)d_in[1], (const char*)d_in[2], (const char*)d_in[3],
                                                   (const char*)d_in[5], (const char*)d_in[6], (const char*)d_in[7], (const char*)d_in[8],
                                                   (char*)d_out, (char*)d_ws);
}
```

```cpp
#define PROF 1
#define OUTAUX 2
#include <hip/hip_runtime.h>
#include <hip/hip_bf16.h>

typedef unsigned u32;
typedef unsigned long long u64;
typedef __bf16 bf16x2 __attribute__((ext_vector_type(2)));
typedef __bf16 bf16x8 __attribute__((ext_vector_type(8)));
typedef short s16x4 __attribute__((ext_vector_type(4)));
typedef float f32x2 __attribute__((ext_vector_type(2)));
typedef float f32x4 __attribute__((ext_vector_type(4)));
typedef float f32x16 __attribute__((ext_vector_type(16)));
typedef u32 u32x2 __attribute__((ext_vector_type(2)));
typedef u32 u32x4 __attribute__((ext_vector_type(4)));
#define LAS __attribute__((address_space(3)))
#define GAS __attribute__((address_space(1)))
#define DI __device__ __forceinline__

#define MAPMODE 0
#define ROTMODE 1
#define CODEPF 1

constexpr u32 WS_FP1 = 4096;
constexpr u32 WS_FH = WS_FP1 + 512 * 128;
constexpr u32 WS_FO = WS_FH + 128 * 128;
constexpr u32 WS_PZ = WS_FO + 128 * 128;
constexpr u32 WS_PKV = WS_PZ + 128 * 256;
constexpr u32 WS_O = WS_PKV + 128 * 4096;
constexpr u32 WS_H = WS_O + 16 * 64 * 2048;
constexpr u32 WS_END = WS_H + 16 * 172 * 2048;
constexpr u32 WS_PROF = 16u << 20;
constexpr u32 OOB = 0x7ff00000u;

constexpr u32 SLOT = 40960;
constexpr u32 L_PHI = 40960;
constexpr u32 L_V = L_PHI + 32768;
constexpr u32 L_SMALLW = 18 * 8192;
constexpr u32 L_SCR = 19 * 8192;
constexpr u32 L_XST = L_SCR;
constexpr u32 L_CS = L_SCR + 4096;
constexpr u32 L_RS = L_SCR + 7168;
constexpr u32 L_KV = L_SCR;
constexpr u32 L_Z = L_SCR + 4096;
constexpr u32 L_XCH = 11 * 8192;

constexpr float LOG2E = 1.4426950408889634f;

DI u32x4 mk_srd(const void* p, u32 bytes) {
    u64 a = (u64)p;
    u32x4 r = {(u32)a, (u32)(a >> 32) & 0xffffu, bytes, 0x00020000u};
    return r;
}
DI void dma16(u32 lds, u32 voff, u32x4 srd, u32 soff) {
    asm volatile("s_mov_b32 m0, %0\n\ts_nop 0\n\tbuffer_load_dwordx4 %1, %2, %3 offen lds"
                 :: "s"(lds), "v"(voff), "s"(srd), "s"(soff) : "memory");
}
#define BARRIER() asm volatile("s_waitcnt lgkmcnt(0)\n\ts_barrier" ::: "memory")
#define WAITV_BARRIER(n) asm volatile("s_waitcnt vmcnt(" #n ") lgkmcnt(0)\n\ts_barrier" ::: "memory")
#define WAITV(n) asm volatile("s_waitcnt vmcnt(" #n ")" ::: "memory")

DI u32 pk2(float a, float b) {
    f32x2 f = {a, b};
    return __builtin_bit_cast(u32, __builtin_convertvector(f, bf16x2));
}
DI bf16x8 asfrag(u32x4 v) { return __builtin_bit_cast(bf16x8, v); }
DI float addf(float a, float b) { float r; asm("v_add_f32 %0, %1, %2" : "=v"(r) : "v"(a), "v"(b)); return r; }
DI float fmacf(float acc, float a) { asm("v_fmac_f32 %0, %1, %1" : "+v"(acc) : "v"(a)); return acc; }
#define MFMA32(a, b, c) __builtin_amdgcn_mfma_f32_32x32x16_bf16((a), (b), (c), 0, 0, 0)
#define MFMA16(a, b, c) __builtin_amdgcn_mfma_f32_16x16x32_bf16((a), (b), (c), 0, 0, 0)

struct Lds {
    LAS char* p;
    DI u32x4 r128(u32 a) const { return *(LAS u32x4*)(p + a); }
    DI f32x4 r128f(u32 a) const { return *(LAS f32x4*)(p + a); }
    DI void w128(u32 a, u32x4 v) const { *(LAS u32x4*)(p + a) = v; }
    DI void w64(u32 a, u32x2 v) const { *(LAS u32x2*)(p + a) = v; }
    DI void w32f(u32 a, float v) const { *(LAS float*)(p + a) = v; }
    DI u32 r16(u32 a) const { return *(LAS unsigned short*)(p + a); }
    DI bf16x8 tr(u32 a, u32 d) const {
        s16x4 lo = __builtin_amdgcn_ds_read_tr16_b64_v4i16((LAS s16x4*)(p + a));
        s16x4 hi = __builtin_amdgcn_ds_read_tr16_b64_v4i16((LAS s16x4*)(p + a + d));
        return __builtin_bit_cast(bf16x8, __builtin_shufflevector(lo, hi, 0, 1, 2, 3, 4, 5, 6, 7));
    }
};

DI void poll_flags(const char* ws, u32 off, bool active, u64 tag) {
    GAS u64* f = (GAS u64*)(ws + off);
    for (u32 spins = 0; spins < (1u << 18); ++spins) {
        u64 v = tag;
        if (active) v = __hip_atomic_load(f, __ATOMIC_RELAXED, __HIP_MEMORY_SCOPE_AGENT);
        if (__all(v == tag)) break;
        __builtin_amdgcn_s_sleep(1);
    }
    asm volatile("" ::: "memory");
}
DI void set_flag(char* ws, u32 off, u64 tag) {
    __hip_atomic_store((GAS u64*)(ws + off), tag, __ATOMIC_RELAXED, __HIP_MEMORY_SCOPE_AGENT);
}

DI void dual_softmax(const f32x16& p, float sc, u32 (&o)[16]) {
    float mx = p[0], mn = p[0];
#pragma unroll
    for (int i = 1; i < 16; ++i) { mx = __builtin_elementwise_maximum(mx, p[i]); mn = __builtin_elementwise_minimum(mn, p[i]); }
    const float c = sc * LOG2E;
    const f32x2 cp = {c, c}, cn = {-c, -c};
    const float bx = -mx * c, bn = mn * c;
    const f32x2 bxp = {bx, bx}, bnp = {bn, bn};
    f32x2 ep[8], en[8];
    f32x2 sp = {0.f, 0.f}, sn = {0.f, 0.f};
#pragma unroll
    for (int i = 0; i < 8; ++i) {
        const f32x2 v = {p[2 * i], p[2 * i + 1]};
        const f32x2 a = __builtin_elementwise_fma(v, cp, bxp), b = __builtin_elementwise_fma(v, cn, bnp);
        ep[i] = f32x2{__builtin_amdgcn_exp2f(a[0]), __builtin_amdgcn_exp2f(a[1])};
        en[i] = f32x2{__builtin_amdgcn_exp2f(b[0]), __builtin_amdgcn_exp2f(b[1])};
        sp += ep[i]; sn += en[i];
    }
    const float rp = __builtin_amdgcn_rcpf(sp[0] + sp[1]), rn = __builtin_amdgcn_rcpf(sn[0] + sn[1]);
    const f32x2 rpp = {rp, rp}, rnp = {rn, rn};
#pragma unroll
    for (int i = 0; i < 8; ++i) {
        const f32x2 a = ep[i] * rpp, b = en[i] * rnp;
        o[i] = pk2(a[0], a[1]); o[8 + i] = pk2(b[0], b[1]);
    }
}
DI bf16x8 pack8(const f32x16& a, int s) {
    u32x4 r = {pk2(a[8 * s], a[8 * s + 1]), pk2(a[8 * s + 2], a[8 * s + 3]), pk2(a[8 * s + 4], a[8 * s + 5]), pk2(a[8 * s + 6], a[8 * s + 7])};
    return asfrag(r);
}

#define STAMP(i) do { __builtin_amdgcn_sched_barrier(0); stamps[i] = __builtin_amdgcn_s_memrealtime(); __builtin_amdgcn_sched_barrier(0); } while (0)

template <bool QKV>
DI void gemm_a(const Lds L, u32 w, u32 l, u32 k0, u32x4 srdx, u32x4 srdw, u32x4 srd2, u32 wtile, const u32 (&vx)[2], const u32 (&vw)[3],
               u32 v2, u32 s2base, f32x16 (&acc)[3], f32x4 (&cs)[3], float& xs, float& xq) {
    const u32 tg = w & 3, hp = w >> 2, r = l & 31, h = l >> 5;
    const u32 rot = tg ^ (2 * hp);
    const u32 q = (l & 15) >> 2, p = l & 3, blk = (l >> 4) & 1, ps = ((p & 1) << 1) | (p >> 1);
    u32 xa[4], wa[4];
#pragma unroll
    for (int i = 0; i < 4; ++i) {
        const u32 s = i ^ rot;
        xa[i] = (32 * tg + r) * 128 + 16 * ((2 * s + h) ^ ((r >> 1) & 7));
        wa[i] = 16384 + (16 * s + 8 * h + q) * 384 + 64 * (hp ^ (q >> 1)) + 32 * blk + 8 * ps;
    }
    const u32 one = ((l & 3) == 0 && ((l >> 2) & 1) == ((l >> 4) & 1)) ? 0x3f803f80u : 0u;
    const bf16x8 sel = asfrag(u32x4{one, one, one, one});
    float s0 = 0.f, s1 = 0.f, q0 = 0.f, q1 = 0.f;
    u32 cur = 0, nxt = 2 * SLOT;
#pragma unroll 1
    for (u32 t = 0; t < 8; ++t) {
        WAITV_BARRIER(5);
        if (t < 6) {
            const u32 kt = (k0 + t + 2) & 7;
            dma16(nxt + w * 2048, vx[0], srdx, kt * 128);
            dma16(nxt + w * 2048 + 1024, vx[1], srdx, kt * 128);
#pragma unroll
            for (int j = 0; j < 3; ++j) dma16(nxt + 16384 + w * 3072 + j * 1024, vw[j], srdw, kt * wtile);
        } else if (t >= 6) {
            const u32 r0 = (t == 6) ? 11 : 0;
#pragma unroll
            for (int j = 0; j < 5; ++j) dma16((r0 + j) * 8192 + w * 1024, v2, srd2, s2base + (r0 + j) * 32768);
        }
#pragma unroll
        for (int i = 0; i < 4; ++i) {
            const u32x4 xr = L.r128(cur + xa[i]);
            const bf16x8 xf = asfrag(xr);
            const bf16x8 w0 = L.tr(cur + wa[i], 4 * 384), w1 = L.tr(cur + wa[i] + 128, 4 * 384), w2 = L.tr(cur + wa[i] + 256, 4 * 384);
            acc[0] = MFMA32(w0, xf, acc[0]);
            acc[1] = MFMA32(w1, xf, acc[1]);
            acc[2] = QKV ? MFMA32(xf, w2, acc[2]) : MFMA32(w2, xf, acc[2]);
            if (i < 2) {
#pragma unroll
                for (int d = 0; d < 4; ++d) {
                    const float lo = __builtin_bit_cast(float, xr[d] << 16), hi = __builtin_bit_cast(float, xr[d] & 0xffff0000u);
                    s0 = addf(s0, lo); s1 = addf(s1, hi); q0 = fmacf(q0, lo); q1 = fmacf(q1, hi);
                }
            }
            if (i == 2) { cs[0] = MFMA16(sel, w0, cs[0]); cs[1] = MFMA16(sel, w1, cs[1]); cs[2] = MFMA16(sel, w2, cs[2]); }
        }
        nxt = cur; cur = (cur == 2 * SLOT) ? 0 : cur + SLOT;
    }
    xs = s0 + s1; xq = q0 + q1;
}

__global__ void __launch_bounds__(512, 2)
hedgehog_mixer_kernel(const char* __restrict__ x, const char* __restrict__ wqkv, const char* __restrict__ wphi, const char* __restrict__ wo,
                      const char* __restrict__ w1, const char* __restrict__ b1, const char* __restrict__ w2, const char* __restrict__ b2,
                      char* __restrict__ out, char* ws) {
    __shared__ __attribute__((aligned(16))) char smem[163840];
    const Lds L{(LAS char*)smem};
    u64 stamps[16];
#pragma unroll
    for (int i = 0; i < 16; ++i) stamps[i] = 0;
    STAMP(0);
    const u64 clk0 = __builtin_amdgcn_s_memtime();
    const u32 b = blockIdx.x, xcd = b & 7, bi = b >> 3;
    const u32 l = threadIdx.x & 63, w = __builtin_amdgcn_readfirstlane(threadIdx.x >> 6);
    {
        u64 pc; u32 len;
        asm volatile("s_getpc_b64 %0\n.Lpf_here:\n\ts_mov_b32 %1, .Lpf_end-.Lpf_here" : "=s"(pc), "=s"(len));
        if (w == 7) {
            const u32x4 sc = mk_srd((const void*)pc, len);
            u32 dummy;
            const u32 off = (l < 8) ? (l * 32 + bi) * 128 : OOB;
            asm volatile("s_nop 4\n\tbuffer_load_dword %0, %1, %2, 0 offen\n\ts_waitcnt vmcnt(0)" : "=&v"(dummy) : "v"(off), "s"(sc) : "memory");
        }
    }
    const u32 rt = (xcd & 1) * 8 + (bi & 7), isq = (bi >> 3) & 1, ci = (xcd >> 1) * 2 + (bi >> 4);
    const u32 k0 = (rt + 2 * ci + isq) & 7;
    const u32 rb = xcd * 4 + (bi & 3), cb = bi >> 2;
    const u32 u0 = ci * 5 + (ci < 3 ? ci : 3), nu = ci < 3 ? 6 : 5;
    const u64 tag = *(const __attribute__((address_space(4))) u64*)(u64)ws + 1;

    const u32x4 srdx = mk_srd(x + rt * 131072, 131072);
    const u32x4 srdw = isq ? mk_srd(wqkv, 512 * 3072) : mk_srd(w1, 512 * 2752);
    const u32 wrow = isq ? 3072 : 2752;
    u32 vx[2], vw[3];
#pragma unroll
    for (int j = 0; j < 2; ++j) vx[j] = (16 * w + 8 * j + (l >> 3)) * 1024 + 16 * ((l & 7) ^ (l >> 4) ^ (4 * j));
#pragma unroll
    for (int j = 0; j < 3; ++j) {
        const u32 o = 64 * j + l, row = o / 24, pos = o % 24, bs = (pos >> 2) ^ ((row >> 1) & 1);
        const u32 col = isq ? (bs >> 1) * 1024 + ci * 128 + (bs & 1) * 64 : (u0 + bs) * 64;
        vw[j] = (!isq && bs >= nu) ? OOB : (8 * w + row) * wrow + col + 16 * (pos & 3);
    }
#pragma unroll
    for (int t = 0; t < 2; ++t) {
        const u32 kt = (k0 + t) & 7;
        dma16(t * SLOT + w * 2048, vx[0], srdx, kt * 128);
        dma16(t * SLOT + w * 2048 + 1024, vx[1], srdx, kt * 128);
#pragma unroll
        for (int j = 0; j < 3; ++j) dma16(t * SLOT + 16384 + w * 3072 + j * 1024, vw[j], srdw, kt * 64 * wrow);
    }
    {
        const u32x4 srds = isq ? mk_srd(wphi + ci * 2048, 2048) : mk_srd(b1 + u0 * 64, nu * 64);
        dma16(L_SMALLW + w * 1024, w * 1024 + l * 16, srds, 0);
    }
    const u32 kh = w >> 2, tq = w & 3;
    const u32x4 srd2 = mk_srd(w2, 1376 * 1024), srdo = mk_srd(wo, 512 * 1024);
    const u32 v2 = (8 * tq + (l >> 3)) * 1024 + cb * 128 + 16 * ((l & 7) ^ (2 * (((l >> 4) & 1) | ((w & 1) << 1))));
    const u32 s2base = kh * 22 * 32768;

    const u32 tg = w & 3, hp = w >> 2, r = l & 31, h = l >> 5;
    f32x16 acc[3]; f32x4 cs[3];
#pragma unroll
    for (int j = 0; j < 3; ++j) { acc[j] = f32x16{}; cs[j] = f32x4{}; }
    float xs, xq;
    STAMP(1);
    if (isq) gemm_a<true>(L, w, l, k0, srdx, srdw, srd2, 64 * 3072, vx, vw, v2, s2base, acc, cs, xs, xq);
    else gemm_a<false>(L, w, l, k0, srdx, srdw, srd2, 64 * 2752, vx, vw, v2, s2base, acc, cs, xs, xq);
    STAMP(2);

    const u32 p4 = (l >> 2) & 3, ps4 = ((p4 & 1) << 1) | (p4 >> 1);
    const u32 feat = (l & 16) + 4 * ps4 + (l & 3);
    L.w64(L_XST + ((tg * 32 + r) * 4 + hp * 2 + h) * 8, u32x2{__builtin_bit_cast(u32, xs), __builtin_bit_cast(u32, xq)});
    if (l < 32) {
#pragma unroll
        for (int j = 0; j < 3; ++j) L.w32f(L_CS + ((hp * 3 + j) * 32 + l) * 16 + tg * 4, cs[j][0]);
    }
    u32 bias[3] = {0, 0, 0};
    bf16x8 wph = {};
    if (isq) {
        const u32 q = (l & 15) >> 2, p = l & 3, blk = (l >> 4) & 1;
        wph = L.tr(L_SMALLW + (2 * hp + (p & 1)) * 512 + (8 * h + q) * 32 + (2 * blk + (p >> 1)) * 8, 128);
    } else {
#pragma unroll
        for (int j = 0; j < 3; ++j) bias[j] = L.r16(L_SMALLW + ((2 * j + hp) * 32 + feat) * 2);
    }
    BARRIER();
#pragma unroll
    for (int j = 0; j < 3; ++j) dma16((16 + j) * 8192 + w * 1024, v2, srd2, s2base + (16 + j) * 32768);
    float rs;
    {
        const f32x4 a = L.r128f(L_XST + (tg * 32 + r) * 32), c = L.r128f(L_XST + (tg * 32 + r) * 32 + 16);
        const float mean = ((a[0] + a[2]) + (c[0] + c[2])) * (1.f / 512.f);
        const float var = fmaxf(((a[1] + a[3]) + (c[1] + c[3])) * (1.f / 512.f) - mean * mean, 0.f);
        rs = __builtin_amdgcn_rsqf(var + 1e-5f);
        if (!isq) rs *= __builtin_amdgcn_rsqf(var * rs * rs + 1e-5f);
        const float sd = __builtin_amdgcn_rcpf(rs);
        const u32 mh = pk2(-mean, 0.f) & 0xffffu, sh = pk2(sd, 0.f) & 0xffffu;
        const u32 ml = pk2(-mean - __builtin_bit_cast(float, mh << 16), 0.f) & 0xffffu;
        const u32 sl = pk2(sd - __builtin_bit_cast(float, sh << 16), 0.f) & 0xffffu;
        const bf16x8 xfix = asfrag(u32x4{mh | (ml << 16), mh | (sh << 16), sl, 0u});
#pragma unroll
        for (int j = 0; j < 3; ++j) {
            const f32x4 cp = L.r128f(L_CS + ((hp * 3 + j) * 32 + r) * 16);
            const float c0 = (cp[0] + cp[1]) + (cp[2] + cp[3]);
            const u32 ch = pk2(c0, 0.f) & 0xffffu, cl = pk2(c0 - __builtin_bit_cast(float, ch << 16), 0.f) & 0xffffu;
            u32x4 wf = {ch | (ch << 16), cl | (bias[j] << 16), bias[j], 0u};
            if (h) wf = u32x4{0u, 0u, 0u, 0u};
            if (j == 2 && isq) acc[j] = MFMA32(xfix, asfrag(wf), acc[j]);
            else acc[j] = MFMA32(asfrag(wf), xfix, acc[j]);
        }
    }
    const auto rws = __builtin_amdgcn_make_buffer_rsrc(ws, 0, WS_END, 0x00020000);
    const auto r2 = __builtin_amdgcn_make_buffer_rsrc((void*)w2, 0, 1376 * 1024, 0x00020000);
    const auto ro = __builtin_amdgcn_make_buffer_rsrc((void*)wo, 0, 512 * 1024, 0x00020000);
    u32x4 raw[11];

    if (!isq) {
        const float ga = -2.f * 0.7978845608028654f * LOG2E * rs, gb = ga * 0.044715f * rs * rs, sdv = __builtin_amdgcn_rcpf(rs);
        const f32x2 gap = {ga, ga}, gbp = {gb, gb}, sdp = {sdv, sdv};
#pragma unroll
        for (int j = 0; j < 3; ++j) {
            dma16((5 + 2 * j) * 8192 + w * 1024, v2, srd2, s2base + (5 + 2 * j) * 32768);
            dma16((6 + 2 * j) * 8192 + w * 1024, v2, srd2, s2base + (6 + 2 * j) * 32768);
            if (2 * j + hp < nu) {
                u32 o[8];
#pragma unroll
                for (int i = 0; i < 8; ++i) {
                    const f32x2 v = {acc[j][2 * i], acc[j][2 * i + 1]};
                    const f32x2 z = v * __builtin_elementwise_fma(v * v, gbp, gap);
                    const f32x2 e = {__builtin_amdgcn_exp2f(z[0]), __builtin_amdgcn_exp2f(z[1])};
                    const f32x2 d = __builtin_elementwise_fma(e, sdp, sdp);
                    const f32x2 y = v * f32x2{__builtin_amdgcn_rcpf(d[0]), __builtin_amdgcn_rcpf(d[1])};
                    o[i] = pk2(y[0], y[1]);
                }
                const u32 ho = WS_H + ((rt * 172 + 4 * (u0 + 2 * j + hp) + h) * 128 + 32 * tg + r) * 16;
                __builtin_amdgcn_raw_buffer_store_b128(u32x4{o[0], o[1], o[2], o[3]}, rws, ho, 0, 16);
                __builtin_amdgcn_raw_buffer_store_b128(u32x4{o[4], o[5], o[6], o[7]}, rws, ho + 4096, 0, 16);
            }
        }
        STAMP(3);
        WAITV_BARRIER(0);
        if (threadIdx.x == 64) set_flag(ws, WS_FH + (rt * 8 + ci) * 128, tag);
        STAMP(4);
#pragma unroll
        for (int j = 0; j < 3; ++j) raw[j] = __builtin_amdgcn_raw_buffer_load_b128(r2, v2, s2base + (19 + j) * 32768, 0);
#pragma unroll
        for (int j = 0; j < 8; ++j) raw[3 + j] = __builtin_amdgcn_raw_buffer_load_b128(ro, v2, (kh * 8 + j) * 32768, 0);
    } else {
        const u32 hm = ((l >> 2) & 1);
        const u32x4 wpu = __builtin_bit_cast(u32x4, wph);
        const u32 m0 = hm ? 0u : ~0u, m1 = ~m0;
        const bf16x8 wp0 = asfrag(wpu & m0), wp1 = asfrag(wpu & m1);
        if (l < 32) L.w32f(L_RS + w * 128 + l * 4, rs);
        {
            f32x16 pk = MFMA32(wp0, pack8(acc[1], 0), f32x16{});
            pk = MFMA32(wp1, pack8(acc[1], 1), pk);
            u32 vp[8];
#pragma unroll
            for (int g = 0; g < 4; ++g) {
                const f32x4 rr = L.r128f(L_RS + w * 128 + (8 * g + 4 * h) * 4);
                vp[2 * g] = pk2(acc[2][4 * g] * rr[0], acc[2][4 * g + 1] * rr[1]);
                vp[2 * g + 1] = pk2(acc[2][4 * g + 2] * rr[2], acc[2][4 * g + 3] * rr[3]);
            }
            L.w128(L_V + w * 2048 + l * 16, u32x4{vp[0], vp[1], vp[2], vp[3]});
            L.w128(L_V + w * 2048 + 1024 + l * 16, u32x4{vp[4], vp[5], vp[6], vp[7]});
            u32 ph[16];
            dual_softmax(pk, rs, ph);
            const u32 pa = L_PHI + ((2 * hp + h) * 128 + 32 * tg + r) * 64;
#pragma unroll
            for (int c = 0; c < 4; ++c) L.w128(pa + 16 * (c ^ ((r >> 1) & 3)), u32x4{ph[4 * c], ph[4 * c + 1], ph[4 * c + 2], ph[4 * c + 3]});
        }
        BARRIER();
        STAMP(3);
        if (w < 4) {
            const u32 q = (l & 15) >> 2, p = l & 3, blk = (l >> 4) & 1, ps = ((p & 1) << 1) | (p >> 1);
            const u32 tok = 4 * h + q;
            const u32 ch = 2 * blk + (ps >> 1);
            const u32 a0 = L_PHI + (w * 128 + tok) * 64 + 16 * (ch ^ ((tok >> 1) & 3)) + 8 * (ps & 1);
            const bool own = blk == (w & 1);
            f32x16 kv = {};
#pragma unroll
            for (int s = 0; s < 8; ++s) {
                const bf16x8 af = L.tr(a0 + s * 1024, 512);
                u32x4 vf = L.r128(L_V + (w >> 1) * 8192 + s * 1024 + l * 16);
                if (!own) vf = u32x4{0x3f803f80u, 0x3f803f80u, 0x3f803f80u, 0x3f803f80u};
                kv = MFMA32(af, asfrag(vf), kv);
            }
            const u32 pb = ci * 16 + rt;
            const u32 ok = WS_PKV + pb * 4096 + (w * 4 + h) * 256 + (feat & 15) * 16, oz = WS_PZ + pb * 256 + (w * 4 + h) * 16;
            const u32 o0 = own ? ok : ((l & 15) == 0 ? oz : OOB), o1 = own ? ok + 512 : ((l & 15) == 0 ? oz + 32 : OOB);
            __builtin_amdgcn_raw_buffer_store_b128(__builtin_bit_cast(u32x4, pack8(kv, 0)), rws, o0, 0, 16);
            __builtin_amdgcn_raw_buffer_store_b128(__builtin_bit_cast(u32x4, pack8(kv, 1)), rws, o1, 0, 16);
        }
        u32 pq[16];
        {
            f32x16 pp = MFMA32(wp0, pack8(acc[0], 0), f32x16{});
            pp = MFMA32(wp1, pack8(acc[0], 1), pp);
            dual_softmax(pp, rs, pq);
        }
        if (w < 4) {
            WAITV(0);
            if (l == 0) set_flag(ws, WS_FP1 + ((ci * 16 + rt) * 4 + w) * 128, tag);
        }
        STAMP(4);
        BARRIER();
#pragma unroll
        for (int j = 5; j < 11; ++j) dma16(j * 8192 + w * 1024, v2, srd2, s2base + j * 32768);
#pragma unroll
        for (int j = 0; j < 3; ++j) raw[j] = __builtin_amdgcn_raw_buffer_load_b128(r2, v2, s2base + (19 + j) * 32768, 0);
#pragma unroll
        for (int j = 0; j < 8; ++j) raw[3 + j] = __builtin_amdgcn_raw_buffer_load_b128(ro, v2, (kh * 8 + j) * 32768, 0);
        if (w == 0) poll_flags(ws, WS_FP1 + (ci * 64 + l) * 128, true, tag);
        BARRIER();
        STAMP(5);
        {
            u32x4 g[8];
            const u32 go = WS_PKV + (ci * 16 + 8 * h) * 4096 + (32 * w + r) * 16;
#pragma unroll
            for (int n = 0; n < 8; ++n) g[n] = __builtin_amdgcn_raw_buffer_load_b128(rws, go, n * 4096, 16);
            const u32x4 zr = __builtin_amdgcn_raw_buffer_load_b128(rws, WS_PZ + (ci * 16 + 2 * w) * 256 + r * 16, 0, 16);
            float sm[8];
#pragma unroll
            for (int j = 0; j < 8; ++j) sm[j] = 0.f;
#pragma unroll
            for (int n = 0; n < 8; ++n)
#pragma unroll
                for (int d = 0; d < 4; ++d) {
                    sm[2 * d] += __builtin_bit_cast(float, g[n][d] << 16);
                    sm[2 * d + 1] += __builtin_bit_cast(float, g[n][d] & 0xffff0000u);
                }
            float t4[4];
#pragma unroll
            for (int j = 0; j < 4; ++j) {
                const auto sw = __builtin_amdgcn_permlane32_swap(__builtin_bit_cast(u32, sm[j]), __builtin_bit_cast(u32, sm[j + 4]), false, false);
                t4[j] = __uint_as_float(sw[0]) + __uint_as_float(sw[1]);
            }
            L.w64(L_KV + (32 * w + r) * 16 + 8 * h, u32x2{pk2(t4[0], t4[1]), pk2(t4[2], t4[3])});
            if (l < 32) L.w128(L_Z + (l & 15) * 256 + (2 * w + (l >> 4)) * 16, zr);
        }
        BARRIER();
        STAMP(6);
        {
            const u32 fa = (2 * hp + h) * 1024 + ((r & 3) + 4 * (r >> 3)) * 16;
            const u32 am = (hm == h) ? ~0u : 0u;
            f32x16 num = {}, den = {};
#pragma unroll
            for (int s = 0; s < 4; ++s) {
                const bf16x8 bq = asfrag(u32x4{pq[4 * s], pq[4 * s + 1], pq[4 * s + 2], pq[4 * s + 3]});
                den = MFMA32(asfrag(L.r128(L_Z + fa + s * 256) & am), bq, den);
                num = MFMA32(asfrag(L.r128(L_KV + fa + s * 256) & am), bq, num);
            }
            f32x2 d2 = {den[0], den[1]};
#pragma unroll
            for (int i = 1; i < 8; ++i) d2 += f32x2{den[2 * i], den[2 * i + 1]};
            const float rd = __builtin_amdgcn_rcpf(d2[0] + d2[1] + 1e-6f);
            num *= rd;
            const u32 oo = WS_O + ((rt * 64 + 2 * (4 * ci + 2 * hp + h)) * 128 + 32 * tg + r) * 16;
            __builtin_amdgcn_raw_buffer_store_b128(__builtin_bit_cast(u32x4, pack8(num, 0)), rws, oo, 0, 16);
            __builtin_amdgcn_raw_buffer_store_b128(__builtin_bit_cast(u32x4, pack8(num, 1)), rws, oo + 2048, 0, 16);
        }
        WAITV_BARRIER(0);
        if (threadIdx.x == 64) set_flag(ws, WS_FO + (rt * 8 + ci) * 128, tag);
        STAMP(7);
    }

    const u32 prt = rb >> 1, g = l >> 4, tk = l & 15;
    const u32 tokl = 64 * (rb & 1) + 16 * tq + tk;
    u32x2 xres[2], bres[2];
#pragma unroll
    for (int j = 0; j < 2; ++j) {
        const u32 col = cb * 64 + 32 * kh + 16 * j + 4 * g;
        xres[j] = *(const u32x2*)(x + (size_t)(prt * 128 + tokl) * 1024 + col * 2);
        bres[j] = *(const u32x2*)(b2 + col * 2);
    }
    if (w == 0) poll_flags(ws, WS_FH + (prt * 8 + (l & 7)) * 128, l < 8, tag);
    WAITV_BARRIER(0);
    STAMP(8);
    u32x4 hf[22];
    {
        const u32 ho = WS_H + ((prt * 172 + 88 * kh + g) * 128 + tokl) * 16;
#pragma unroll
        for (int j = 0; j < 22; ++j) hf[j] = __builtin_amdgcn_raw_buffer_load_b128(rws, (j == 21 && kh) ? OOB : ho, j * 8192, 16);
    }
    u32 ba[4];
    {
        const u32 q = (l & 15) >> 2, p = l & 3, swz = (q >> 1) | ((g & 1) << 1);
#pragma unroll
        for (int c = 0; c < 4; ++c) ba[c] = kh * 4096 + (8 * g + q) * 128 + 32 * (c ^ swz) + 8 * p;
    }
    f32x4 am[4], as[4];
#pragma unroll
    for (int c = 0; c < 4; ++c) { am[c] = f32x4{}; as[c] = f32x4{}; }
#pragma unroll
    for (int j = 0; j < 22; ++j) {
        if (j == 11) {
            BARRIER();
#pragma unroll
            for (int n = 0; n < 11; ++n) L.w128(n * 8192 + w * 1024 + l * 16, raw[n]);
        }
        if (j == 19) BARRIER();
        const u32 sl = (j < 19 ? j : j - 19) * 8192;
#pragma unroll
        for (int c = 0; c < 4; ++c) am[c] = MFMA16(L.tr(sl + ba[c], 512), asfrag(hf[j]), am[c]);
    }
    STAMP(9);
    const u32 xo = L_XCH + w * 4096 + l * 16, xi = L_XCH + (w ^ 4) * 4096 + l * 16;
    L.w128(xo, __builtin_bit_cast(u32x4, kh ? am[0] : am[2]));
    L.w128(xo + 1024, __builtin_bit_cast(u32x4, kh ? am[1] : am[3]));
    if (w == 0) {
        poll_flags(ws, WS_FO + (prt * 8 + (l & 7)) * 128, l < 8, tag);
        if (b == 0) {
            poll_flags(ws, WS_FH + l * 128, true, tag);
            poll_flags(ws, WS_FH + (64 + l) * 128, true, tag);
            if (l == 0) set_flag(ws, 0, tag);
        }
    }
    BARRIER();
    STAMP(10);
    {
        u32x4 of[8];
        const u32 oo = WS_O + ((prt * 64 + 32 * kh + g) * 128 + tokl) * 16;
#pragma unroll
        for (int j = 0; j < 8; ++j) of[j] = __builtin_amdgcn_raw_buffer_load_b128(rws, oo, j * 8192, 16);
#pragma unroll
        for (int j = 0; j < 8; ++j)
#pragma unroll
            for (int c = 0; c < 4; ++c) as[c] = MFMA16(L.tr((3 + j) * 8192 + ba[c], 512), asfrag(of[j]), as[c]);
    }
    L.w128(xo + 2048, __builtin_bit_cast(u32x4, kh ? as[0] : as[2]));
    L.w128(xo + 3072, __builtin_bit_cast(u32x4, kh ? as[1] : as[3]));
    f32x4 gate[2];
#pragma unroll
    for (int j = 0; j < 2; ++j) {
        const f32x4 mo = L.r128f(xi + j * 1024), mm = kh ? am[2 + j] : am[j];
#pragma unroll
        for (int e = 0; e < 4; ++e) {
            const u32 bw = bres[j][e >> 1];
            const float m = mo[e] + mm[e] + __builtin_bit_cast(float, (e & 1) ? (bw & 0xffff0000u) : (bw << 16));
            gate[j][e] = m * __builtin_amdgcn_rcpf(1.f + __builtin_amdgcn_exp2f(-LOG2E * m));
        }
    }
    BARRIER();
    STAMP(11);
#pragma unroll
    for (int j = 0; j < 2; ++j) {
        const f32x4 so = L.r128f(xi + 2048 + j * 1024), sm = kh ? as[2 + j] : as[j];
        float y[4];
#pragma unroll
        for (int e = 0; e < 4; ++e) {
            const u32 xw = xres[j][e >> 1];
            y[e] = __builtin_bit_cast(float, (e & 1) ? (xw & 0xffff0000u) : (xw << 16)) + (so[e] + sm[e]) * gate[j][e];
        }
        const u32 col = cb * 64 + 32 * kh + 16 * j + 4 * g;
        __builtin_amdgcn_raw_buffer_store_b64(u32x2{pk2(y[0], y[1]), pk2(y[2], y[3])}, __builtin_amdgcn_make_buffer_rsrc(out, 0, 2048 * 1024, 0x00020000),
                                              (prt * 128 + tokl) * 1024 + col * 2, 0, OUTAUX);
    }
    asm volatile(".Lpf_end:");
    STAMP(12);
    WAITV(0);
    STAMP(13);
    if (threadIdx.x == 0) {
        u64* st = (u64*)(ws + WS_PROF) + ((tag & 15) * 256 + b) * 16;
        stamps[14] = isq; stamps[15] = (__builtin_amdgcn_s_getreg(20 | (3 << 11)) & 0xf) | ((__builtin_amdgcn_s_memtime() - clk0) << 8);
#pragma unroll
        for (int i = 0; i < 16; ++i) st[i] = stamps[i];
    }
}

extern "C" void kernel_launch(void* const* d_in, const int* in_sizes, int n_in, void* d_out, int out_size, void* d_ws, size_t ws_size,
                              hipStream_t stream) {
    hedgehog_mixer_kernel<<<256, 512, 0, stream>>>((const char*)d_in[0], (const char*)d_in[1], (const char*)d_in[2], (const char*)d_in[3],
                                                   (const char*)d_in[5], (const char*)d_in[6], (const char*)d_in[7], (const char*)d_in[8],
                                                   (char*)d_out, (char*)d_ws);
}
```

```cpp
#include <hip/hip_runtime.h>
#include <hip/hip_bf16.h>

typedef unsigned u32;
typedef unsigned long long u64;
typedef __bf16 bf16x2 __attribute__((ext_vector_type(2)));
typedef __bf16 bf16x8 __attribute__((ext_vector_type(8)));
typedef short s16x4 __attribute__((ext_vector_type(4)));
typedef float f32x2 __attribute__((ext_vector_type(2)));
typedef float f32x4 __attribute__((ext_vector_type(4)));
typedef float f32x16 __attribute__((ext_vector_type(16)));
typedef u32 u32x2 __attribute__((ext_vector_type(2)));
typedef u32 u32x4 __attribute__((ext_vector_type(4)));
typedef u32 u32x16 __attribute__((ext_vector_type(16)));
#define LAS __attribute__((address_space(3)))
#define DI __device__ __forceinline__
#define SB() __builtin_amdgcn_sched_barrier(0)

#define PROF 0
#define PROFW 0
#define MAPMODE 0
#define ROTMODE 1

constexpr u32 WS_FP = 4096;
constexpr u32 WS_FH = WS_FP + 128 * 128;
constexpr u32 WS_FO = WS_FH + 128 * 128;
constexpr u32 WS_PZ = WS_FO + 128 * 128;
constexpr u32 WS_PKV = WS_PZ + 8 * 4096;
constexpr u32 WS_O = WS_PKV + 128 * 4096;
constexpr u32 WS_H = WS_O + 16 * 64 * 2048;
constexpr u32 WS_END = WS_H + 16 * 172 * 2048;
constexpr u32 WS_PROF = 16u << 20;
constexpr u32 OOB = 0x7ff00000u;

constexpr u32 L_W = 65536;
constexpr u32 L_PHI = 10 * 8192;
constexpr u32 L_V = 14 * 8192;
constexpr u32 L_SCR = 19 * 8192;
constexpr u32 L_XST = L_SCR;
constexpr u32 L_CS = L_SCR + 2048;
constexpr u32 L_SW = L_SCR + 5120;
constexpr u32 L_ZERO = L_SCR + 7168;
constexpr u32 L_KV = L_SCR;
constexpr u32 L_XCH = 11 * 8192;

constexpr float LOG2E = 1.4426950408889634f;

DI u32x4 mk_srd(u32 lo, u32 hi, u32 bytes) { return u32x4{lo, hi & 0xffffu, bytes, 0x00020000u}; }
DI void dma16(u32 lds, u32 voff, u32x4 srd, u32 soff) {
    asm volatile("s_mov_b32 m0, %0\n\ts_nop 0\n\tbuffer_load_dwordx4 %1, %2, %3 offen lds"
                 :: "s"(lds), "v"(voff), "s"(srd), "s"(soff) : "memory");
}
DI void ld16(u32x4& d, u32 voff, u32x4 srd, u32 soff) {
    asm volatile("buffer_load_dwordx4 %0, %1, %2, %3 offen" : "=v"(d) : "v"(voff), "s"(srd), "s"(soff) : "memory");
}
DI void ld16c(u32x4& d, u32 voff, u32x4 srd, u32 soff) {
    asm volatile("buffer_load_dwordx4 %0, %1, %2, %3 offen sc1" : "=v"(d) : "v"(voff), "s"(srd), "s"(soff) : "memory");
}
DI void ld8c(u32x2& d, u32 voff, u32x4 srd) {
    asm volatile("buffer_load_dwordx2 %0, %1, %2, 0 offen sc1" : "=v"(d) : "v"(voff), "s"(srd) : "memory");
}
typedef __amdgpu_buffer_rsrc_t rsrc_t;
DI void st16c(u32x4 v, rsrc_t r, u32 voff) { __builtin_amdgcn_raw_buffer_store_b128(v, r, voff, 0, 16); }
DI void st8c(u32x2 v, rsrc_t r, u32 voff) { __builtin_amdgcn_raw_buffer_store_b64(v, r, voff, 0, 16); }
#define BARRIER() asm volatile("s_waitcnt lgkmcnt(0)\n\ts_barrier" ::: "memory")
#define WAITV_BARRIER(n) asm volatile("s_waitcnt vmcnt(%0) lgkmcnt(0)\n\ts_barrier" :: "n"(n) : "memory")
#define WAITV_FOR(n, r) asm volatile("s_waitcnt vmcnt(%1)" : "+v"(r) : "n"(n) : "memory")
#define PIN(r) asm volatile("" : "+v"(r))

DI u32 pk2(float a, float b) {
    f32x2 f = {a, b};
    return __builtin_bit_cast(u32, __builtin_convertvector(f, bf16x2));
}
DI bf16x8 asfrag(u32x4 v) { return __builtin_bit_cast(bf16x8, v); }
DI float addf(float a, float b) { float r; asm("v_add_f32 %0, %1, %2" : "=v"(r) : "v"(a), "v"(b)); return r; }
DI float fmacf(float acc, float a) { asm("v_fmac_f32 %0, %1, %1" : "+v"(acc) : "v"(a)); return acc; }
#define MFMA32(a, b, c) __builtin_amdgcn_mfma_f32_32x32x16_bf16((a), (b), (c), 0, 0, 0)
#define MFMA16(a, b, c) __builtin_amdgcn_mfma_f32_16x16x32_bf16((a), (b), (c), 0, 0, 0)

struct Lds {
    LAS char* p;
    DI u32x4 r128(u32 a) const { return *(LAS u32x4*)(p + a); }
    DI f32x4 r128f(u32 a) const { return *(LAS f32x4*)(p + a); }
    DI void w128(u32 a, u32x4 v) const { *(LAS u32x4*)(p + a) = v; }
    DI void w64(u32 a, u32x2 v) const { *(LAS u32x2*)(p + a) = v; }
    DI void w32f(u32 a, float v) const { *(LAS float*)(p + a) = v; }
    DI u32 r16(u32 a) const { return *(LAS unsigned short*)(p + a); }
    DI bf16x8 tr2(u32 a, u32 b) const {
        s16x4 lo = __builtin_amdgcn_ds_read_tr16_b64_v4i16((LAS s16x4*)(p + a));
        s16x4 hi = __builtin_amdgcn_ds_read_tr16_b64_v4i16((LAS s16x4*)(p + b));
        return __builtin_bit_cast(bf16x8, __builtin_shufflevector(lo, hi, 0, 1, 2, 3, 4, 5, 6, 7));
    }
    DI bf16x8 tr(u32 a, u32 d) const { return tr2(a, a + d); }
};

DI void poll(u32x2 f, u32 off, u32x4 srd, u32x2 tag) {
#pragma unroll 1
    for (u32 spins = 0; spins < (1u << 18); ++spins) {
        if (__all(f[0] == tag[0] && f[1] == tag[1])) break;
        __builtin_amdgcn_s_sleep(1);
        ld8c(f, off, srd);
        WAITV_FOR(0, f);
    }
}

DI void dual_softmax(const f32x16& p, float sc, u32 (&o)[16]) {
    float mx = p[0], mn = p[0];
#pragma unroll
    for (int i = 1; i < 16; ++i) { mx = __builtin_elementwise_maximum(mx, p[i]); mn = __builtin_elementwise_minimum(mn, p[i]); }
    const float c = sc * LOG2E;
    const f32x2 cp = {c, c}, cn = {-c, -c};
    const float bx = -mx * c, bn = mn * c;
    const f32x2 bxp = {bx, bx}, bnp = {bn, bn};
    f32x2 ep[8], en[8];
    f32x2 sp, sn;
#pragma unroll
    for (int i = 0; i < 8; ++i) {
        const f32x2 v = {p[2 * i], p[2 * i + 1]};
        const f32x2 a = __builtin_elementwise_fma(v, cp, bxp), b = __builtin_elementwise_fma(v, cn, bnp);
        ep[i] = f32x2{__builtin_amdgcn_exp2f(a[0]), __builtin_amdgcn_exp2f(a[1])};
        en[i] = f32x2{__builtin_amdgcn_exp2f(b[0]), __builtin_amdgcn_exp2f(b[1])};
        if (i == 0) { sp = ep[0]; sn = en[0]; } else { sp += ep[i]; sn += en[i]; }
    }
    const float rp = __builtin_amdgcn_rcpf(sp[0] + sp[1]), rn = __builtin_amdgcn_rcpf(sn[0] + sn[1]);
    const f32x2 rpp = {rp, rp}, rnp = {rn, rn};
#pragma unroll
    for (int i = 0; i < 8; ++i) {
        const f32x2 a = ep[i] * rpp, b = en[i] * rnp;
        o[i] = pk2(a[0], a[1]); o[8 + i] = pk2(b[0], b[1]);
    }
}
DI u32x4 pack8(const f32x16& a, int s) {
    return u32x4{pk2(a[8 * s], a[8 * s + 1]), pk2(a[8 * s + 2], a[8 * s + 3]), pk2(a[8 * s + 4], a[8 * s + 5]), pk2(a[8 * s + 6], a[8 * s + 7])};
}
DI void ln_fix(const Lds L, u32 csa, u32 bias, u32 hmask, bf16x8 xfix, f32x16& acc) {
    const f32x4 cp = L.r128f(csa);
    const float c0 = (cp[0] + cp[1]) + (cp[2] + cp[3]);
    const u32 chh = pk2(c0, c0);
    const u32 cl = pk2(c0 - __uint_as_float(chh << 16), 0.f) & 0xffffu;
    const u32x4 wf = {chh & hmask, (cl | (bias << 16)) & hmask, bias & hmask, 0u};
    acc = MFMA32(asfrag(wf), xfix, acc);
}
template <class FS, class FM>
DI void pass2(const Lds L, const u32 (&wb)[4], const u32x4 (&xf)[32], bf16x8 sel, f32x16& acc, f32x4& cs, FS step, FM mid) {
    bf16x8 f[3];
    f[0] = L.tr(wb[0], 512); f[1] = L.tr(wb[1], 512);
#pragma unroll
    for (int n = 0; n < 32; ++n) {
        if (n == 14) mid();
        if (n + 2 < 32) f[(n + 2) % 3] = L.tr(wb[(n + 2) & 3] + 8192 * ((n + 2) >> 2), 512);
        SB();
        acc = MFMA32(f[n % 3], asfrag(xf[n]), acc);
        if ((n & 3) == 2) cs = MFMA16(sel, f[n % 3], cs);
        SB();
        step(n);
        SB();
    }
}

#define STAMP(i) do {} while (0)

__global__ void __launch_bounds__(512, 2)
hedgehog_mixer_kernel(const void* x_, const void* wqkv_, const void* w1_, void* ws_, const void* w2_, const void* wo_, const void* wphi_,
                      const void* b1_, const void* b2_, void* out_) {
    __shared__ __attribute__((aligned(16))) char smem[163840];
    const Lds L{(LAS char*)smem};
    u32x16 ka; u32x4 kb;
    {
        const u64 kp = (u64)__builtin_amdgcn_kernarg_segment_ptr();
        asm volatile("s_load_dwordx16 %0, %2, 0\n\ts_load_dwordx4 %1, %2, 64" : "=&s"(ka), "=&s"(kb) : "s"(kp));
    }
    const u32 b = blockIdx.x, xcd = b & 7, bi = b >> 3;
    const u32 l = threadIdx.x & 63, w = __builtin_amdgcn_readfirstlane(threadIdx.x >> 6);
    {
        u64 pc; u32 len;
        asm volatile("s_getpc_b64 %0\n.Lpf_here:\n\ts_mov_b32 %1, .Lpf_end-.Lpf_here" : "=s"(pc), "=s"(len));
        const u32x4 sc = mk_srd((u32)pc, (u32)(pc >> 32), len);
        asm volatile("s_mov_b32 m0, %0\n\ts_nop 3\n\tbuffer_load_dword off, %1, %2 lds" :: "n"(L_ZERO), "s"(sc), "s"((w * 32 + bi) * 128) : "memory");
    }
    const u32 rt = (xcd & 1) * 8 + (bi & 7), isq = (bi >> 3) & 1, ci = (xcd >> 1) * 2 + (bi >> 4);
    const u32 k0 = (bi + 2 * (bi >> 3)) & 7;
    const u32 rb = xcd * 4 + (bi & 3), cb = bi >> 2;
    const u32 tg = w & 3, hp = w >> 2, r = l & 31, h = l >> 5;
    const u32 wrow = isq ? 3072 : 2752, wt = wrow * 64;
    const u32 l3 = l >> 3, csw = 16 * ((l & 7) ^ (4 * ((l >> 4) & 1)));
    u32 vx0 = (rt * 128 + 16 * w + l3) * 1024 + 16 * ((l & 7) ^ (l >> 4)), vx1 = (vx0 ^ 64) + 8192;
    u32 vw = (8 * w + (l3 & 3)) * wrow + (isq ? 1024 + ci * 128 : ci * 256) + h * (isq ? 1024 : 128) + csw;
    const u32 c3 = (isq ? 0 : 2048) + ci * 128 + csw;
    const u32 vw3 = c3 < wrow ? (8 * w + l3) * wrow + c3 : OOB;
    const u32 v2 = (8 * tg + l3) * 1024 + cb * 128 + 16 * ((l & 7) ^ (2 * (((l >> 4) & 1) | ((w & 1) << 1))));
    const u32 s2b = hp * 22 * 32768;
    PIN(vx0); PIN(vx1); PIN(vw);
    asm volatile("s_waitcnt lgkmcnt(0)" : "+s"(ka), "+s"(kb));
    u64 epoch;
    asm volatile("s_load_dwordx2 %0, %1, 0" : "=s"(epoch) : "s"((u64)ka[6] | ((u64)ka[7] << 32)));
    const u32x4 srdx = mk_srd(ka[0], ka[1], 2048 * 1024);
    const u32x4 srdw = mk_srd(isq ? ka[2] : ka[4], isq ? ka[3] : ka[5], 512 * wrow);
    const u32x4 srdws = mk_srd(ka[6], ka[7], WS_END);
    const u32x4 srd2 = mk_srd(ka[8], ka[9], 1376 * 1024), srdo = mk_srd(ka[10], ka[11], 512 * 1024);
    auto cell = [&](u32 c) { dma16(c * 8192 + w * 1024, v2, srd2, s2b + c * 32768); };
    auto w3 = [&](u32 t) { dma16(t * 8192 + w * 1024, vw3, srdw, ((k0 + t) & 7) * wt); };
    auto piece = [&](int n, int p) {
        if (n < 8) {
            const u32 kt = (k0 + n) & 7, s = (n & 3) * 16384 + w * 2048 + (p & 1) * 1024;
            if (p < 2) dma16(s, p ? vx1 : vx0, srdx, kt * 128);
            else dma16(L_W + s, vw, srdw, kt * wt + (p & 1) * 4 * wrow);
        } else if (p < 2) w3(2 * (n - 8) + p);
        else if (n == 8) cell(6 + p);
        else if (2 * n + p < 23) cell(2 * n + p - 4);
    };
#pragma unroll
    for (int n = 0; n < 3; ++n)
#pragma unroll
        for (int p = 0; p < 4; ++p) piece(n, p);
    {
        const u32x4 srds = mk_srd(isq ? ka[12] : ka[14], isq ? ka[13] : ka[15], isq ? 16384 : 2752);
        const u32 so = ci * (isq ? 2048 : (w ? 128 : 256)) + w * (isq ? 256 : 2048);
        asm volatile("s_mov_b32 m0, %0\n\ts_nop 0\n\tbuffer_load_dword %1, %2, %3 offen lds"
                     :: "s"(L_SW + w * 256), "v"(l * 4), "s"(srds), "s"(so) : "memory");
    }
    const u32 q = (l & 15) >> 2, p = l & 3, blk = (l >> 4) & 1, ps = ((p & 1) << 1) | (p >> 1);
    const u32 rot = tg ^ (2 * hp), inner = q * 128 + 64 * (hp ^ (q >> 1)) + 32 * blk + 8 * ps;
    u32 xa[4], wa[4];
#pragma unroll
    for (int i = 0; i < 4; ++i) {
        const u32 s = i ^ rot;
        xa[i] = (32 * tg + r) * 128 + 16 * ((2 * s + h) ^ ((r >> 1) & 7));
        wa[i] = L_W + (4 * s + 2 * h) * 1024 + inner;
    }
    const u32 one = ((l & 3) == 0 && ((l >> 2) & 1) == ((l >> 4) & 1)) ? 0x3f803f80u : 0u;
    const bf16x8 sel = asfrag(u32x4{one, one, one, one});
    STAMP(1);

    u32x4 xf[32];
    f32x16 acc0 = {}, acc1 = {};
    f32x4 cs0 = {}, cs1 = {};
    float s0 = 0.f, s1 = 0.f, q0 = 0.f, q1 = 0.f;
    asm volatile("s_waitcnt vmcnt(9) lgkmcnt(0)\n\ts_barrier" : "+s"(epoch) :: "memory");
    STAMP(2);
    {
        bf16x8 wc[2][2];
        xf[0] = L.r128(xa[0]);
        wc[0][0] = L.tr(wa[0], 1024); wc[0][1] = L.tr(wa[0] + 512, 1024);
        piece(3, 0);
        auto ln = [&](u32 d) {
            const float lo = __uint_as_float(d << 16), hi = __uint_as_float(d & 0xffff0000u);
            s0 = addf(s0, lo); s1 = addf(s1, hi); q0 = fmacf(q0, lo); q1 = fmacf(q1, hi);
        };
#pragma unroll
        for (int n = 0; n < 32; ++n) {
            const int t = n >> 2, i = n & 3, m = n + 1;
            if (i == 3 && t < 7) { if (t == 0) WAITV_BARRIER(9); else WAITV_BARRIER(8); }
            if (m < 32) {
                const u32 so = ((m >> 2) & 3) * 16384;
                xf[m] = L.r128(xa[m & 3] + so);
                wc[m & 1][0] = L.tr(wa[m & 3] + so, 1024); wc[m & 1][1] = L.tr(wa[m & 3] + so + 512, 1024);
            }
            SB();
            acc0 = MFMA32(wc[n & 1][0], asfrag(xf[n]), acc0);
            SB();
            ln(xf[4 * t + (i >> 1)][2 * (i & 1)]);
            if (m < 32) piece((m >> 2) + 3, m & 3);
            SB();
            acc1 = MFMA32(wc[n & 1][1], asfrag(xf[n]), acc1);
            SB();
            ln(xf[4 * t + (i >> 1)][2 * (i & 1) + 1]);
            if (i == 2) { cs0 = MFMA16(sel, wc[n & 1][0], cs0); cs1 = MFMA16(sel, wc[n & 1][1], cs1); }
            SB();
        }
    }
    STAMP(3);
    const u32x2 tag = {(u32)(epoch + 1), (u32)((epoch + 1) >> 32)};
    {
        const auto sw = __builtin_amdgcn_permlane32_swap(__float_as_uint(s0 + s1), __float_as_uint(q0 + q1), false, false);
        L.w32f(L_XST + (tg * 32 + r) * 16 + hp * 8 + h * 4, __uint_as_float(sw[0]) + __uint_as_float(sw[1]));
        const auto sc = __builtin_amdgcn_permlane32_swap(__float_as_uint(cs0[0]), __float_as_uint(cs1[0]), false, false);
        L.w32f(L_CS + ((h * 2 + hp) * 32 + r) * 16 + tg * 4, __uint_as_float(sc[0]));
    }
    const u32 feat = (l & 16) + 4 * ((((l >> 2) & 1) << 1) | ((l >> 3) & 1)) + (l & 3);
    u32 bias[3];
#pragma unroll
    for (int j = 0; j < 3; ++j) bias[j] = L.r16(L_SW + ((2 * j + hp) * 32 + feat) * 2) & (isq - 1);
    WAITV_BARRIER(5);
    w3(6); w3(7);
    float rs;
    bf16x8 xfix;
    const u32 hmask = h - 1, csa = L_CS + (hp * 32 + r) * 16;
    {
        const f32x4 a = L.r128f(L_XST + (tg * 32 + r) * 16);
        const float mean = (a[0] + a[2]) * (1.f / 512.f);
        const float var = fmaxf((a[1] + a[3]) * (1.f / 512.f) - mean * mean, 0.f);
        rs = __builtin_amdgcn_rsqf(var + 1e-5f);
        if (!isq) rs *= __builtin_amdgcn_rsqf(var * rs * rs + 1e-5f);
        const float sd = __builtin_amdgcn_rcpf(rs);
        const u32 hs = pk2(-mean, sd);
        const u32 ls = pk2(-mean - __uint_as_float(hs << 16), sd - __uint_as_float(hs & 0xffff0000u));
        xfix = asfrag(u32x4{(hs & 0xffffu) | (ls << 16), hs, ls >> 16, 0u});
        ln_fix(L, csa, bias[0], hmask, xfix, acc0);
        ln_fix(L, csa + 1024, bias[1], hmask, xfix, acc1);
    }
    u32 wb[4];
#pragma unroll
    for (int i = 0; i < 4; ++i) wb[i] = (4 * (i ^ rot) + 2 * h) * 512 + inner;
    const rsrc_t rws = __builtin_amdgcn_make_buffer_rsrc((void*)((u64)ka[6] | ((u64)ka[7] << 32)), 0, WS_END, 0x00020000);
    u32x4 raw[11];
    auto rawld = [&](int j) { if (j < 3) ld16(raw[j], v2, srd2, s2b + (19 + j) * 32768); else ld16(raw[j], v2, srdo, (hp * 8 + j - 3) * 32768); };
    const u32 prt = rb >> 1;
    const u32 fho = WS_FH + (prt * 8 + (l & 7)) * 128;
    u32x2 fl;
    auto publish = [&](u32 flag) {
        ld8c(fl, fho, srdws);
        asm volatile("s_waitcnt vmcnt(0) lgkmcnt(0)\n\ts_barrier"
                     : "+v"(fl), "+v"(raw[0]), "+v"(raw[1]), "+v"(raw[2]), "+v"(raw[3]), "+v"(raw[4]), "+v"(raw[5]), "+v"(raw[6]), "+v"(raw[7]),
                       "+v"(raw[8]), "+v"(raw[9]), "+v"(raw[10]) :: "memory");
        if (w == 1) st8c(tag, rws, l == 0 ? flag : OOB);
    };
    f32x16 acc2 = {};
    f32x4 cs2 = {};

    if (!isq) {
        const float ga = -2.f * 0.7978845608028654f * LOG2E * rs, gb = ga * 0.044715f * rs * rs, sdv = __builtin_amdgcn_rcpf(rs);
        const u32 ho = WS_H + ((rt * 172 + 16 * ci + 4 * hp + h) * 128 + 32 * tg + r) * 16;
        float e0, e1; u32x4 o;
        pass2(L, wb, xf, sel, acc2, cs2, [&](int n) {
            const int i = n & 14;
            const float v0 = n < 16 ? acc0[i] : acc1[i], v1 = n < 16 ? acc0[i + 1] : acc1[i + 1];
            if (!(n & 1)) {
                e0 = __builtin_amdgcn_exp2f(v0 * fmaf(v0 * v0, gb, ga)); e1 = __builtin_amdgcn_exp2f(v1 * fmaf(v1 * v1, gb, ga));
            } else {
                float y0 = v0 * __builtin_amdgcn_rcpf(fmaf(e0, sdv, sdv)), y1 = v1 * __builtin_amdgcn_rcpf(fmaf(e1, sdv, sdv));
                PIN(y0); PIN(y1);
                o[(n >> 1) & 3] = pk2(y0, y1);
                if ((n & 7) == 7) st16c(o, rws, ho + (n >> 4) * 16384 + ((n >> 3) & 1) * 4096);
                if (n < 12) cell(10 + (n >> 1));
                else if (n >= 15 && n < 23) cell((n - 15) >> 1);
                else if (n >= 23) rawld((n - 23) >> 1);
            }
        }, [&]() { WAITV_BARRIER(7); });
        STAMP(4);
        if (l < 32) L.w32f(csa + 2048 + tg * 4, cs2[0]);
        BARRIER();
        ln_fix(L, csa + 2048, bias[2], hmask, xfix, acc2);
        const f32x2 gap = {ga, ga}, gbp = {gb, gb}, sdp = {sdv, sdv};
        const u32 h2 = (2 * ci + hp < 11) ? WS_H + ((rt * 172 + 128 + 8 * ci + 4 * hp + h) * 128 + 32 * tg + r) * 16 : OOB;
#pragma unroll
        for (int i = 0; i < 8; ++i) {
            const f32x2 v = {acc2[2 * i], acc2[2 * i + 1]};
            const f32x2 z = v * __builtin_elementwise_fma(v * v, gbp, gap);
            const f32x2 e = {__builtin_amdgcn_exp2f(z[0]), __builtin_amdgcn_exp2f(z[1])};
            const f32x2 d = __builtin_elementwise_fma(e, sdp, sdp);
            const f32x2 y = v * f32x2{__builtin_amdgcn_rcpf(d[0]), __builtin_amdgcn_rcpf(d[1])};
            o[i & 3] = pk2(y[0], y[1]);
            if ((i & 3) == 3) st16c(o, rws, h2 + (i >> 2) * 4096);
        }
        STAMP(5);
        WAITV_BARRIER(0);
        if (w == 1) st8c(tag, rws, l == 0 ? WS_FH + (rt * 8 + ci) * 128 : OOB);
        STAMP(6);
#pragma unroll
        for (int j = 4; j < 8; ++j) cell(j);
#pragma unroll
        for (int j = 5; j < 11; ++j) rawld(j);
        publish(OOB);
#pragma unroll
        for (int n = 0; n < 32; ++n) asm volatile("" : "=v"(xf[n]));
        asm volatile("" : "=v"(acc0), "=v"(acc1));
    }
    u32 isq2 = isq;
    asm volatile("" : "+s"(isq2));
    if (isq2) {
        acc2 = f32x16{}; cs2 = f32x4{};
        const u32 hm = (l >> 2) & 1;
        const u32x4 wpu = __builtin_bit_cast(u32x4, L.tr(L_SW + (2 * hp + (p & 1)) * 512 + (8 * h + q) * 32 + (2 * blk + (p >> 1)) * 8, 128));
        u32 m0 = hm - 1; PIN(m0);
        const bf16x8 wp0 = asfrag(wpu & m0), wp1 = asfrag(wpu & ~m0);
        if (l < 4 && w == 0) L.w128(L_ZERO + l * 256, u32x4{0u, 0u, 0u, 0u});
        {
            f32x16 pk = MFMA32(wp0, asfrag(pack8(acc0, 0)), f32x16{});
            pk = MFMA32(wp1, asfrag(pack8(acc0, 1)), pk);
            acc1 *= rs;
            const u32 va = L_V + (32 * tg + r) * 128 + 16 * ((4 * hp + h) ^ (r & 7));
            L.w128(va, pack8(acc1, 0));
            L.w128(va ^ 32, pack8(acc1, 1));
            u32 ph[16];
            dual_softmax(pk, rs, ph);
            const u32 pa = L_PHI + ((2 * hp + h) * 128 + 32 * tg + r) * 64 + 16 * ((r >> 1) & 3);
#pragma unroll
            for (int c = 0; c < 4; ++c) L.w128(pa ^ (16 * c), u32x4{ph[4 * c], ph[4 * c + 1], ph[4 * c + 2], ph[4 * c + 3]});
        }
        BARRIER();
        STAMP(4);
        if (w < 4) {
            const u32 tok = 8 * h + q;
            const u32 a0 = L_PHI + (w * 128 + tok) * 64 + 16 * ((2 * blk + (ps >> 1)) ^ (q >> 1)) + 8 * (ps & 1), a1 = (a0 ^ 32) + 256;
            const u32 b0 = L_V + tok * 128 + 16 * ((2 * w + (p >> 1)) ^ q) + 8 * (p & 1), b1 = (b0 ^ 64) + 512;
            u32 vm = blk - 1; PIN(vm);
            const u32 vo = 0x3f803f80u & ~vm;
            f32x16 kv = {};
#pragma unroll
            for (int s = 0; s < 8; ++s) {
                const bf16x8 af = L.tr2(a0 + s * 1024, a1 + s * 1024);
                const u32x4 vf = (__builtin_bit_cast(u32x4, L.tr2(b0 + s * 2048, b1 + s * 2048)) & vm) | vo;
                kv = MFMA32(af, asfrag(vf), kv);
            }
            const u32 ok = WS_PKV + (ci * 16 + rt) * 4096 + (w * 4 + h) * 256 + r * 16, oz = WS_PZ + ci * 4096 + (w * 4 + h) * 256 + rt * 16;
            const u32 o0 = r < 16 ? ok : (r == 16 ? oz : OOB);
            st16c(pack8(kv, 0), rws, o0);
            st16c(pack8(kv, 1), rws, o0 + 512);
        }
        pass2(L, wb, xf, sel, acc2, cs2, [&](int n) {
            if (n < 14) { if (!(n & 1)) rawld(n >> 1); }
            else if (n == 15) cell(10);
            else if (!(n & 1)) { const int c = (n - 14) >> 1; cell(c < 5 ? 11 + c : c - 5); }
        }, [&]() {
            WAITV_BARRIER(7);
            if (w == 1) st8c(tag, rws, l == 0 ? WS_FP + (ci * 16 + rt) * 128 : OOB);
        });
        STAMP(5);
        if (l < 32) L.w32f(csa + 2048 + tg * 4, cs2[0]);
        BARRIER();
#pragma unroll
        for (int j = 4; j < 8; ++j) cell(j);
        ln_fix(L, csa + 2048, 0, hmask, xfix, acc2);
        u32 pq[16];
        {
            f32x16 pp = MFMA32(wp0, asfrag(pack8(acc2, 0)), f32x16{});
            pp = MFMA32(wp1, asfrag(pack8(acc2, 1)), pp);
            dual_softmax(pp, rs, pq);
#pragma unroll
            for (int i = 0; i < 16; ++i) PIN(pq[i]);
        }
        STAMP(6);
        if (w == 0) poll(u32x2{~tag[0], 0u}, WS_FP + (ci * 16 + (l & 15)) * 128, srdws, tag);
        BARRIER();
        STAMP(7);
        const u32 dr = ((r & 3) + 4 * (r >> 3)) * 16;
        float rd;
        {
            u32x4 zf[4], g[8];
            const u32 zo = hm == h ? WS_PZ + ci * 4096 + (2 * hp + h) * 1024 + dr : OOB;
            const u32 go = WS_PKV + (ci * 16 + 8 * h) * 4096 + (32 * w + r) * 16;
#pragma unroll
            for (int s = 0; s < 4; ++s) ld16c(zf[s], zo, srdws, s * 256);
#pragma unroll
            for (int n = 0; n < 8; ++n) ld16c(g[n], go, srdws, n * 4096);
#pragma unroll
            for (int j = 7; j < 11; ++j) rawld(j);
            asm volatile("s_waitcnt vmcnt(12)" : "+v"(zf[0]), "+v"(zf[1]), "+v"(zf[2]), "+v"(zf[3]) :: "memory");
            f32x16 den = {};
#pragma unroll
            for (int s = 0; s < 4; ++s) den = MFMA32(asfrag(zf[s]), asfrag(u32x4{pq[4 * s], pq[4 * s + 1], pq[4 * s + 2], pq[4 * s + 3]}), den);
            float sm[8];
            u32 c_lo = 0x00003f80u, c_hi = 0x3f800000u;
            asm("" : "+s"(c_lo), "+s"(c_hi));
            const bf16x2 elo = __builtin_bit_cast(bf16x2, c_lo), ehi = __builtin_bit_cast(bf16x2, c_hi);
            SB();
#pragma unroll
            for (int n = 0; n < 8; ++n) {
                WAITV_FOR(11 - n, g[n]);
#pragma unroll
                for (int d = 0; d < 4; ++d) {
                    const u32 gd = g[n][d];
                    if (n == 0) { sm[2 * d] = __uint_as_float(gd << 16); sm[2 * d + 1] = __uint_as_float(gd & 0xffff0000u); }
                    else {
                        sm[2 * d] = __builtin_amdgcn_fdot2_f32_bf16(__builtin_bit_cast(bf16x2, gd), elo, sm[2 * d], false);
                        sm[2 * d + 1] = __builtin_amdgcn_fdot2_f32_bf16(__builtin_bit_cast(bf16x2, gd), ehi, sm[2 * d + 1], false);
                    }
                }
                SB();
            }
            float t4[4];
#pragma unroll
            for (int j = 0; j < 4; ++j) {
                const auto sw = __builtin_amdgcn_permlane32_swap(__float_as_uint(sm[j]), __float_as_uint(sm[j + 4]), false, false);
                t4[j] = __uint_as_float(sw[0]) + __uint_as_float(sw[1]);
            }
            L.w64(L_KV + (32 * w + r) * 16 + 8 * h, u32x2{pk2(t4[0], t4[1]), pk2(t4[2], t4[3])});
            f32x2 d2 = {den[0], den[1]};
#pragma unroll
            for (int i = 1; i < 8; ++i) d2 += f32x2{den[2 * i], den[2 * i + 1]};
            rd = __builtin_amdgcn_rcpf(d2[0] + d2[1] + 1e-6f);
            PIN(rd);
        }
        BARRIER();
        STAMP(8);
        {
            const u32 fa = hm == h ? L_KV + (2 * hp + h) * 1024 + dr : L_ZERO;
            f32x16 num = {};
#pragma unroll
            for (int s = 0; s < 4; ++s) num = MFMA32(asfrag(L.r128(fa + s * 256)), asfrag(u32x4{pq[4 * s], pq[4 * s + 1], pq[4 * s + 2], pq[4 * s + 3]}), num);
            num *= rd;
            const u32 oo = WS_O + ((rt * 64 + 2 * (4 * ci + 2 * hp + h)) * 128 + 32 * tg + r) * 16;
            st16c(pack8(num, 0), rws, oo);
            st16c(pack8(num, 1), rws, oo + 2048);
        }
        publish(WS_FO + (rt * 8 + ci) * 128);
    }

    const u32 g = l >> 4, tk = l & 15;
    const u32 tokl = 64 * (rb & 1) + 16 * tg + tk;
    poll(fl, fho, srdws, tag);
    STAMP(9);
    u32x4 hf[22], xr, br;
    const u32 colo = cb * 128 + 64 * hp + 32 * (g & 1) + 16 * (g >> 1), outo = (prt * 128 + tokl) * 1024 + colo;
    {
        const u32 ho = WS_H + ((prt * 172 + 88 * hp + g) * 128 + tokl) * 16;
#pragma unroll
        for (int j = 0; j < 22; ++j) ld16c(hf[j], ho, srdws, j < 21 ? j * 8192 : (hp ? OOB : 21 * 8192));
        ld16(xr, outo, srdx, 0);
        ld16(br, colo, mk_srd(kb[0], kb[1], 1024), 0);
    }
    u32 ba[4], bw[4];
    {
        const u32 swz = (q >> 1) | ((g & 1) << 1);
#pragma unroll
        for (int c = 0; c < 4; ++c) ba[c] = hp * 4096 + (8 * g + q) * 128 + 32 * (c ^ swz) + 8 * p;
    }
    f32x4 am[4], as[4];
#pragma unroll
    for (int c = 0; c < 4; ++c) { am[c] = f32x4{}; as[c] = f32x4{}; }
    {
        bf16x8 f[2][4];
#pragma unroll
        for (int c = 0; c < 4; ++c) f[0][c] = L.tr(ba[c], 512);
#pragma unroll
        for (int j = 0; j < 22; ++j) {
            if (j == 10 || j == 18) BARRIER();
            if (j < 21) {
                const int sl = j + 1 < 19 ? j + 1 : j - 18;
#pragma unroll
                for (int c = 0; c < 4; ++c) {
                    if (sl == 8 || sl == 16) { bw[c] = ba[c] + sl * 8192; PIN(bw[c]); }
                    f[(j + 1) & 1][c] = L.tr((sl & 7) * 8192 + (sl < 8 ? ba[c] : bw[c]), 512);
                }
            }
            WAITV_FOR(23 - j, hf[j]);
            SB();
#pragma unroll
            for (int c = 0; c < 4; ++c) am[c] = MFMA16(f[j & 1][c], asfrag(hf[j]), am[c]);
            SB();
            if (j >= 10 && j < 16) {
                L.w128((2 * j - 20) * 8192 + w * 1024 + l * 16, raw[2 * j - 20]);
                if (j < 15) L.w128((2 * j - 19) * 8192 + w * 1024 + l * 16, raw[2 * j - 19]);
            }
            SB();
        }
    }
    STAMP(10);
    const u32 foo = WS_FO + (prt * 8 + 4 * hp + (l & 3)) * 128;
    ld8c(fl, foo, srdws);
    const u32 xo = L_XCH + w * 4096 + l * 16, xi = L_XCH + (w ^ 4) * 4096 + l * 16;
    L.w128(xo, __builtin_bit_cast(u32x4, hp ? am[0] : am[2]));
    L.w128(xo + 1024, __builtin_bit_cast(u32x4, hp ? am[1] : am[3]));
    bf16x8 wf[8][4];
#pragma unroll
    for (int j = 0; j < 8; ++j)
#pragma unroll
        for (int c = 0; c < 4; ++c) {
            if (j == 0) { bw[c] = ba[c] + 3 * 8192; PIN(bw[c]); }
            wf[j][c] = L.tr(j * 8192 + bw[c], 512);
        }
    asm volatile("s_waitcnt vmcnt(0) lgkmcnt(0)\n\ts_barrier" : "+v"(fl), "+v"(xr), "+v"(br) :: "memory");
    f32x4 gate[2];
    u32 xres[4];
    {
        const auto b0 = __builtin_amdgcn_permlane16_swap(br[0], br[2], false, false), b1 = __builtin_amdgcn_permlane16_swap(br[1], br[3], false, false);
        const auto x0 = __builtin_amdgcn_permlane16_swap(xr[0], xr[2], false, false), x1 = __builtin_amdgcn_permlane16_swap(xr[1], xr[3], false, false);
        xres[0] = x0[0]; xres[1] = x1[0]; xres[2] = x0[1]; xres[3] = x1[1];
#pragma unroll
        for (int j = 0; j < 2; ++j) {
            const f32x4 mo = L.r128f(xi + j * 1024), mm = hp ? am[2 + j] : am[j];
#pragma unroll
            for (int e = 0; e < 4; ++e) {
                const u32 bw = (e >> 1) ? b1[j] : b0[j];
                const float m = mo[e] + mm[e] + __uint_as_float((e & 1) ? (bw & 0xffff0000u) : (bw << 16));
                gate[j][e] = m * __builtin_amdgcn_rcpf(1.f + __builtin_amdgcn_exp2f(-LOG2E * m));
            }
            PIN(gate[j]);
        }
    }
    poll(fl, foo, srdws, tag);
    STAMP(11);
    {
        u32x4 of[8];
        const u32 oo = WS_O + ((prt * 64 + 32 * hp + g) * 128 + tokl) * 16;
#pragma unroll
        for (int j = 0; j < 8; ++j) ld16c(of[j], oo, srdws, j * 8192);
#pragma unroll
        for (int j = 0; j < 8; ++j) {
            WAITV_FOR(7 - j, of[j]);
#pragma unroll
            for (int c = 0; c < 4; ++c) as[c] = MFMA16(wf[j][c], asfrag(of[j]), as[c]);
            SB();
        }
    }
    L.w128(xo + 2048, __builtin_bit_cast(u32x4, hp ? as[0] : as[2]));
    L.w128(xo + 3072, __builtin_bit_cast(u32x4, hp ? as[1] : as[3]));
    BARRIER();
    STAMP(12);
    {
        u32 y[4];
#pragma unroll
        for (int j = 0; j < 2; ++j) {
            const f32x4 sv = L.r128f(xi + 2048 + j * 1024) + (hp ? as[2 + j] : as[j]);
#pragma unroll
            for (int e = 0; e < 2; ++e) {
                const u32 xw = xres[2 * j + e];
                y[2 * j + e] = pk2(fmaf(sv[2 * e], gate[j][2 * e], __uint_as_float(xw << 16)),
                                   fmaf(sv[2 * e + 1], gate[j][2 * e + 1], __uint_as_float(xw & 0xffff0000u)));
            }
        }
        const auto y0 = __builtin_amdgcn_permlane16_swap(y[0], y[2], false, false), y1 = __builtin_amdgcn_permlane16_swap(y[1], y[3], false, false);
        const rsrc_t ro = __builtin_amdgcn_make_buffer_rsrc((void*)((u64)kb[2] | ((u64)kb[3] << 32)), 0, 2048 * 1024, 0x00020000);
        __builtin_amdgcn_raw_buffer_store_b128(u32x4{y0[0], y1[0], y0[1], y1[1]}, ro, outo, 0, 0);
    }
    if (b == 0 && w == 0) {
        poll(u32x2{~tag[0], 0u}, WS_FH + l * 128, srdws, tag);
        poll(u32x2{~tag[0], 0u}, WS_FH + (64 + l) * 128, srdws, tag);
        st8c(tag, rws, l == 0 ? 0 : OOB);
    }
    asm volatile(".Lpf_end:");
}

extern "C" void kernel_launch(void* const* d_in, const int* in_sizes, int n_in, void* d_out, int out_size, void* d_ws, size_t ws_size,
                              hipStream_t stream) {
    hedgehog_mixer_kernel<<<256, 512, 0, stream>>>(d_in[0], d_in[1], d_in[5], d_ws, d_in[7], d_in[3], d_in[2], d_in[6], d_in[8], d_out);
}
```

```cpp
#include <hip/hip_runtime.h>
#include <hip/hip_bf16.h>
#include <type_traits>

typedef unsigned u32;
typedef unsigned long long u64;
typedef __bf16 bf16x2 __attribute__((ext_vector_type(2)));
typedef __bf16 bf16x8 __attribute__((ext_vector_type(8)));
typedef __bf16 bf16x16 __attribute__((ext_vector_type(16)));
typedef short s16x4 __attribute__((ext_vector_type(4)));
typedef float f32x2 __attribute__((ext_vector_type(2)));
typedef float f32x4 __attribute__((ext_vector_type(4)));
typedef float f32x16 __attribute__((ext_vector_type(16)));
typedef u32 u32x2 __attribute__((ext_vector_type(2)));
typedef u32 u32x4 __attribute__((ext_vector_type(4)));
typedef u32 u32x16 __attribute__((ext_vector_type(16)));
#define LAS __attribute__((address_space(3)))
#define DI __device__ __forceinline__
#define SB() __builtin_amdgcn_sched_barrier(0)
template <int N> using IC = std::integral_constant<int, N>;
template <int A, int B, class F> DI void rep(F f) { if constexpr (A < B) { f(IC<A>{}); rep<A + 1, B>(f); } }

#define PROF 0
#define PROFW 0
#define ROTMODE 8
#define RPOS 2
#define PRIO 1
#define MIDB 14

constexpr u32 WS_FP = 4096;
constexpr u32 WS_FH = WS_FP + 128 * 128;
constexpr u32 WS_FG = WS_FH + 128 * 128;
constexpr u32 WS_FO = WS_FG + 128 * 128;
constexpr u32 WS_ST = WS_FO + 128 * 128;
constexpr u32 REC = 4352;
constexpr u32 WS_PKV = WS_ST + 2048 * 16;
constexpr u32 WS_O = WS_PKV + 128 * REC;
constexpr u32 WS_H = WS_O + 16 * 64 * 2048;
constexpr u32 WS_END = WS_H + 16 * 172 * 2048;
constexpr u32 WS_PROF = 16u << 20;
constexpr u32 OOB = 0x7ff00000u;
static_assert(WS_O % 2048 == 0, "");

constexpr u32 L_W = 65536;
constexpr u32 L_PHI = 13 * 8192;
constexpr u32 L_V = 17 * 8192;
constexpr u32 L_SCR = 19 * 8192;
constexpr u32 L_B1 = L_SCR;
constexpr u32 L_CS = L_SCR + 2048;
constexpr u32 L_SW = L_SCR + 5120;
constexpr u32 L_ZERO = L_SCR + 7168;
constexpr u32 L_KV = L_SCR;
constexpr u32 L_XM = 2 * 8192;
constexpr u32 L_XS = 16 * 8192;

constexpr float LOG2E = 1.4426950408889634f;

enum { T, ROW, TCH, SWL, B1L, SST, SLD, W3, CELL, ECELL, RAW, HST, KST, GA, HL, OST, FL, XR, BR, OL };
struct Op { int at, kind, arg; };
#define TILE(t, a, b, c, d) {a, T, 4 * t}, {b, T, 4 * t + 1}, {c, T, 4 * t + 2}, {d, T, 4 * t + 3}
constexpr int ST_AT = RPOS == 0 ? -1 : (RPOS == 1 ? 6 : 14);
#define COMMON_OPS \
    {RPOS == 0 ? -3 : (RPOS == 1 ? -2 : -1), ROW, 0}, {ST_AT, SST, 0}, \
    TILE(0, -2, -2, -2, -2), TILE(1, -1, -1, -1, -1), TILE(2, 0, 2, 4, 5), TILE(3, 8, 9, 10, 11), TILE(4, 12, 13, 13, 14), \
    {11, TCH, 0}, {11, SWL, 0}, {11, B1L, 0}, \
    TILE(5, 16, 18, 20, 21), TILE(6, 24, 26, 28, 29), {30, ECELL, 16}, {31, SLD, 0}, TILE(7, 32, 34, 36, 37), {38, ECELL, 17}, \
    {40, W3, 0}, {42, W3, 1}, {44, CELL, 8}, {45, CELL, 9}, {46, ECELL, 18}, {48, W3, 2}, {50, W3, 3}, {52, CELL, 10}, {53, CELL, 11}, \
    {56, W3, 4}, {58, W3, 5}, {60, CELL, 12}, {61, W3, 6}, {63, W3, 7}
#define B1_H(s, j) {400 + 8 * (s) + 1, HL, j}, {400 + 8 * (s) + 5, HL, (j) + 1}
constexpr Op YOPS[] = { COMMON_OPS,
    {100, CELL, 13}, {101, CELL, 14}, {102, CELL, 15}, {103, HST, 0}, {105, RAW, 0}, {106, RAW, 1}, {107, HST, 1}, {109, RAW, 2}, {110, RAW, 3},
    {111, HST, 2}, {113, RAW, 4}, {115, HST, 3},
    {201, RAW, 5}, {203, RAW, 6}, {205, RAW, 7}, {207, RAW, 8}, {209, RAW, 9}, {211, RAW, 10}, {213, RAW, 11},
    {215, RAW, 12}, {217, RAW, 13}, {219, RAW, 14}, {220, CELL, 0}, {222, CELL, 1}, {224, CELL, 2}, {226, CELL, 3},
    {230, GA, 0}, {230, GA, 1}, {231, GA, 2}, {231, GA, 3}, {300, GA, 4}, {300, GA, 5}, {301, GA, 6}, {301, GA, 7},
    {310, HL, 0}, {311, HL, 1}, {312, HL, 2}, {313, HL, 3}, {314, HL, 4}, {315, HL, 5}, {316, HL, 6}, {317, HL, 7},
    {330, OST, 0}, {330, OST, 1},
    B1_H(0, 8), B1_H(1, 10), {415, FL, 0}, B1_H(2, 12), B1_H(3, 14), B1_H(4, 16), B1_H(5, 18), B1_H(6, 20), {455, FL, 1},
    {457, XR, 0}, {461, BR, 0},
    {472, OL, 0}, {474, OL, 1}, {476, OL, 2}, {478, OL, 3}, {480, OL, 4}, {482, OL, 5}, {484, OL, 6}, {486, OL, 7} };
constexpr int Y_FG = 4, Y_FO = 9;
constexpr Op XOPS[] = { COMMON_OPS,
    {102, RAW, 0}, {103, RAW, 1}, {104, RAW, 2}, {105, RAW, 3}, {150, KST, 0},
    {203, RAW, 4}, {205, RAW, 5}, {207, RAW, 6}, {209, RAW, 7}, {211, RAW, 8}, {213, RAW, 9},
    {215, CELL, 13}, {217, CELL, 14}, {219, CELL, 15}, {221, CELL, 0}, {223, CELL, 1}, {225, CELL, 2}, {227, CELL, 3}, {229, RAW, 10},
    {230, HL, 0}, {231, HL, 1}, {300, HL, 2}, {301, HL, 3}, {302, HL, 4}, {303, HL, 5}, {304, HL, 6}, {305, HL, 7}, {307, HST, 0}, {307, HST, 1},
    {310, CELL, 4}, {310, CELL, 5}, {403, CELL, 6}, {407, CELL, 7},
    B1_H(0, 8), B1_H(1, 10), B1_H(2, 12), B1_H(3, 14), {433, CELL, 16}, {437, CELL, 17}, {441, CELL, 18}, {445, XR, 0}, {447, BR, 0}, {451, FL, 0},
    {456, HL, 16}, {457, HL, 17}, {458, HL, 18}, {459, HL, 19}, {461, HL, 20}, {463, HL, 21}, {479, FL, 1},
    {500, OL, 0}, {500, OL, 1}, {500, OL, 2}, {500, OL, 3}, {500, OL, 4}, {500, OL, 5}, {500, OL, 6}, {500, OL, 7} };
constexpr int X_FG = 7, X_FO = 11;
template <bool X> constexpr int nops() { return X ? sizeof(XOPS) / sizeof(Op) : sizeof(YOPS) / sizeof(Op); }
template <bool X> constexpr Op opat(int i) { return X ? XOPS[i] : YOPS[i]; }
constexpr bool is_store(int k) { return k == SST || k == HST || k == KST || k == OST; }
template <bool X> constexpr long long okey(int i) { return (opat<X>(i).at * 4LL + (is_store(opat<X>(i).kind) ? 2 : (opat<X>(i).kind == ROW ? 1 : 0))) * 1024 + i; }
template <bool X> constexpr int after(int kind, int arg, int slot) {
    int me = -1, c = 0;
    for (int i = 0; i < nops<X>(); ++i) if (opat<X>(i).kind == kind && opat<X>(i).arg == arg) me = i;
    if (me < 0 || opat<X>(me).at >= slot) return -1000;
    for (int i = 0; i < nops<X>(); ++i) if (okey<X>(i) > okey<X>(me) && opat<X>(i).at < slot) ++c;
    return c;
}
constexpr int cmin(int a, int b) { return a < b ? a : b; }
template <bool X> constexpr int nth(int slot, int j) {
    int c = 0;
    for (int p = 0; p < 2; ++p)
        for (int i = 0; i < nops<X>(); ++i)
            if (opat<X>(i).at == slot && !is_store(opat<X>(i).kind) && (opat<X>(i).kind == ROW) == p && c++ == j) return i;
    return -1 - c;
}

DI u32x4 mk_srd(u32 lo, u32 hi, u32 bytes) { return u32x4{lo, hi & 0xffffu, bytes, 0x00020000u}; }
DI u32 seq(u32 x, u32 c, u32 a, u32 b) {
    u32 r;
    asm("s_cmp_eq_u32 %1, %2\n\ts_cselect_b32 %0, %3, %4" : "=s"(r) : "s"(x), "n"(c), "s"(a), "s"(b) : "scc");
    return r;
}
template <u32 A, u32 M> DI u32 wrap(u32 f) {
    u32 r;
    asm volatile("s_add_u32 %0, %1, %2\n\ts_and_b32 %0, %0, %3" : "=s"(r) : "s"(f), "n"(A), "n"(M) : "scc");
    return r;
}
template <u32 I> DI void dma16(u32 lb, u32 voff, u32x4 srd, u32 soff) {
    asm volatile("s_add_u32 m0, %0, %4\n\ts_nop 0\n\tbuffer_load_dwordx4 %1, %2, %3 offen lds"
                 :: "s"(lb), "v"(voff), "s"(srd), "s"(soff), "n"(I) : "memory", "scc");
}
template <u32 I> DI void dma16a(u32 lb, u32 voff, u32x4 srd, u32 sa, u32 sb) {
    u32 t;
    asm volatile("s_add_u32 m0, %1, %6\n\ts_add_u32 %0, %4, %5\n\tbuffer_load_dwordx4 %2, %3, %0 offen lds"
                 : "=&s"(t) : "s"(lb), "v"(voff), "s"(srd), "s"(sa), "s"(sb), "n"(I) : "memory", "scc");
}
template <u32 I> DI u32 dma16m(u32 lb, u32 voff, u32x4 srd, u32 sa, u32 sb) {
    u32 t;
    asm volatile("s_add_u32 m0, %1, %6\n\ts_mul_i32 %0, %4, %5\n\tbuffer_load_dwordx4 %2, %3, %0 offen lds"
                 : "=&s"(t) : "s"(lb), "v"(voff), "s"(srd), "s"(sa), "s"(sb), "n"(I) : "memory", "scc");
    return t;
}
template <u32 I, u32 A, u32 M> DI u32 dma16w(u32 lb, u32 voff, u32x4 srd, u32 f) {
    u32 t;
    asm volatile("s_add_u32 m0, %1, %5\n\ts_add_u32 %0, %4, %6\n\ts_and_b32 %0, %0, %7\n\tbuffer_load_dwordx4 %2, %3, %0 offen lds"
                 : "=&s"(t) : "s"(lb), "v"(voff), "s"(srd), "s"(f), "n"(I), "n"(A), "n"(M) : "memory", "scc");
    return t;
}
DI void dma4(u32 lds, u32 voff, u32x4 srd, u32 soff) {
    asm volatile("s_mov_b32 m0, %0\n\ts_nop 0\n\tbuffer_load_dword %1, %2, %3 offen lds" :: "s"(lds), "v"(voff), "s"(srd), "s"(soff) : "memory");
}
DI void ld16(u32x4& d, u32 voff, u32x4 srd, u32 soff) {
    asm volatile("buffer_load_dwordx4 %0, %1, %2, %3 offen" : "=v"(d) : "v"(voff), "s"(srd), "s"(soff) : "memory");
}
DI void ld16c(u32x4& d, u32 voff, u32x4 srd, u32 soff) {
    asm volatile("buffer_load_dwordx4 %0, %1, %2, %3 offen sc1" : "=v"(d) : "v"(voff), "s"(srd), "s"(soff) : "memory");
}
DI void ld8c(u64& d, u32 voff, u32x4 srd) {
    asm volatile("buffer_load_dwordx2 %0, %1, %2, 0 offen sc1" : "=v"(d) : "v"(voff), "s"(srd) : "memory");
}
typedef __amdgpu_buffer_rsrc_t rsrc_t;
DI void st16c(u32x4 v, rsrc_t r, u32 voff) { __builtin_amdgcn_raw_buffer_store_b128(v, r, voff, 0, 16); }
#define BARRIER() asm volatile("s_waitcnt lgkmcnt(0)\n\ts_barrier" ::: "memory")
template <int N> DI void waitv_barrier() { static_assert(N >= 0 && N < 64, ""); asm volatile("s_waitcnt vmcnt(%0) lgkmcnt(0)\n\ts_barrier" :: "n"(N) : "memory"); }
template <int N, class V> DI void waitv(V& r) { static_assert(N >= 0 && N < 64, ""); asm volatile("s_waitcnt vmcnt(%1)" : "+v"(r) : "n"(N) : "memory"); }
template <class V> DI void PIN(V& r) { asm volatile("" : "+v"(r)); }
template <int N, class V> DI void waitv(V& r, V& s) { static_assert(N >= 0 && N < 64, ""); asm volatile("s_waitcnt vmcnt(%2)" : "+v"(r), "+v"(s) : "n"(N) : "memory"); }
template <int P> DI void prio() { if (PRIO) asm volatile("s_setprio %0" :: "n"(P)); }
template <int K> DI void flag(u32 w, u64 tag, u32x4 srd, u32 soff) {
    u64 t;
    asm volatile("s_cmp_lg_u32 %1, %5\n\ts_cbranch_scc1 .Lf%=\n\ts_mov_b64 exec, 1\n\tv_mov_b64 %0, %2\n\t"
                 "buffer_store_dwordx2 %0, off, %3, %4 sc1\n\ts_mov_b64 exec, -1\n.Lf%=:"
                 : "=&v"(t) : "s"(w), "s"(tag), "s"(srd), "s"(soff), "n"(K) : "memory", "scc");
}
#define POLL_LOOP(o) "v_cmp_ne_u64 vcc, %[tag], %[f]\n\ts_cbranch_vccz .Ld%=\n\ts_mov_b32 m0, 0x40000\n.Lp%=:\n\ts_sleep 1\n\t" \
    "buffer_load_dwordx2 %[f], %[off], %[srd], 0 offen offset:" #o " sc1\n\ts_sub_u32 m0, m0, 1\n\ts_waitcnt vmcnt(0)\n\ts_cbranch_scc1 .Le%=\n\t" \
    "v_cmp_ne_u64 vcc, %[tag], %[f]\n\ts_cbranch_vccnz .Lp%=\n.Le%=:\n\t"
template <int N> DI void poll(u64 f, u32 off, u32x4 srd, u64 tag) {
    static_assert(N >= 0 && N < 64, "");
    asm volatile("s_waitcnt vmcnt(%[n])\n\t" POLL_LOOP(0) "\n.Ld%=:" : [f] "+v"(f) : [off] "v"(off), [srd] "s"(srd), [tag] "s"(tag), [n] "n"(N) : "memory", "scc", "vcc");
}
DI void ld8c_w0(u64& f, u32 w, u32 off, u32x4 srd) {
    asm volatile("s_cmp_lg_u32 %1, 0\n\ts_cbranch_scc1 .Ld%=\n\tbuffer_load_dwordx2 %0, %2, %3, 0 offen sc1\n.Ld%=:"
                 : "=v"(f) : "s"(w), "v"(off), "s"(srd) : "memory", "scc");
}
template <int N> DI void poll_w0(u64 f, u32 w, u32 off, u32x4 srd, u64 tag) {
    asm volatile("s_cmp_lg_u32 %[w], 0\n\ts_cbranch_scc1 .Ld%=\n\ts_waitcnt vmcnt(%[n])\n\t" POLL_LOOP(0) "\n.Ld%=:"
                 : [f] "+v"(f) : [w] "s"(w), [off] "v"(off), [srd] "s"(srd), [tag] "s"(tag), [n] "n"(N) : "memory", "scc", "vcc");
}
DI void poll16(u32x4& st, u32 off, u32x4 srd, u64 tag) {
    u64 f = (u64)st[2] | ((u64)st[3] << 32);
    asm volatile(POLL_LOOP(8) "buffer_load_dwordx4 %[st], %[off], %[srd], 0 offen sc1\n\ts_waitcnt vmcnt(0)\n.Ld%=:"
                 : [f] "+v"(f), [st] "+v"(st) : [off] "v"(off), [srd] "s"(srd), [tag] "s"(tag) : "memory", "scc", "vcc");
}

DI u32 pk2(float a, float b) {
    f32x2 f = {a, b};
    return __builtin_bit_cast(u32, __builtin_convertvector(f, bf16x2));
}
DI bf16x8 asfrag(u32x4 v) { return __builtin_bit_cast(bf16x8, v); }
DI bf16x16 cat(bf16x8 a, bf16x8 b) { return __builtin_shufflevector(a, b, 0, 1, 2, 3, 4, 5, 6, 7, 8, 9, 10, 11, 12, 13, 14, 15); }
#define MFMA32(a, b, c) __builtin_amdgcn_mfma_f32_32x32x16_bf16((a), (b), (c), 0, 0, 0)
#define MFMA16(a, b, c) __builtin_amdgcn_mfma_f32_16x16x32_bf16((a), (b), (c), 0, 0, 0)
#define SMFMA(a, b, c, i) __builtin_amdgcn_smfmac_f32_16x16x64_bf16((a), (b), (c), (i), 0, 0)

struct Lds {
    LAS char* p;
    DI u32x4 r128(u32 a) const { return *(LAS u32x4*)(p + a); }
    DI f32x4 r128f(u32 a) const { return *(LAS f32x4*)(p + a); }
    DI void w128(u32 a, u32x4 v) const { *(LAS u32x4*)(p + a) = v; }
    DI void w128f(u32 a, f32x4 v) const { *(LAS f32x4*)(p + a) = v; }
    DI void w64(u32 a, u32x2 v) const { *(LAS u32x2*)(p + a) = v; }
    DI void w32f(u32 a, float v) const { *(LAS float*)(p + a) = v; }
    DI u32 r16(u32 a) const { return *(LAS unsigned short*)(p + a); }
    DI s16x4 trh(u32 a) const { return __builtin_amdgcn_ds_read_tr16_b64_v4i16((LAS s16x4*)(p + a)); }
    static DI bf16x8 join(s16x4 lo, s16x4 hi) { return __builtin_bit_cast(bf16x8, __builtin_shufflevector(lo, hi, 0, 1, 2, 3, 4, 5, 6, 7)); }
    DI bf16x8 tr(u32 a, u32 d) const { return join(trh(a), trh(a + d)); }
};

template <class F> DI void dual_exp(const f32x16& p, float sc, f32x2 (&ep)[8], f32x2 (&en)[8], F hook) {
    float mx = p[0], mn = p[0];
#pragma unroll
    for (int i = 1; i < 16; ++i) { mx = __builtin_elementwise_maximum(mx, p[i]); mn = __builtin_elementwise_minimum(mn, p[i]); }
    const float c = sc * LOG2E;
    const f32x2 cp = {c, c}, cn = {-c, -c};
    const float bx = -mx * c, bn = mn * c;
    const f32x2 bxp = {bx, bx}, bnp = {bn, bn};
    rep<0, 8>([&](auto I) {
        constexpr int i = I;
        const f32x2 v = {p[2 * i], p[2 * i + 1]};
        const f32x2 a = __builtin_elementwise_fma(v, cp, bxp), b = __builtin_elementwise_fma(v, cn, bnp);
        ep[i] = f32x2{__builtin_amdgcn_exp2f(a[0]), __builtin_amdgcn_exp2f(a[1])};
        en[i] = f32x2{__builtin_amdgcn_exp2f(b[0]), __builtin_amdgcn_exp2f(b[1])};
        hook(I);
    });
}
DI u32x4 pack8(const f32x16& a, int s) {
    return u32x4{pk2(a[8 * s], a[8 * s + 1]), pk2(a[8 * s + 2], a[8 * s + 3]), pk2(a[8 * s + 4], a[8 * s + 5]), pk2(a[8 * s + 6], a[8 * s + 7])};
}
DI bf16x8 fix_operand(f32x4 cp, u32 bias, u32 hmask) {
    float a = cp[0] + cp[1]; PIN(a);
    a += cp[2]; PIN(a);
    const float c0 = a + cp[3];
    const u32 chh = pk2(c0, c0);
    const u32 cl = pk2(c0 - __uint_as_float(chh << 16), 0.f);
    u32x4 wf = {chh & hmask, (cl | (bias << 16)) & hmask, bias & hmask, 0u};
    PIN(wf);
    return asfrag(wf);
}
template <int C> DI float dpp_add(float v) {
    return v + __int_as_float(__builtin_amdgcn_update_dpp(0, __float_as_int(v), C, 15, 15, true));
}

#define STAMPOP "s_memrealtime"
#define SSET 0
#define STAMPX(set, i) do {} while (0)
#define STAMP(i) STAMPX(0, i)

__global__ void __launch_bounds__(512, 2)
hedgehog_mixer_kernel(const void* x_, const void* wqkv_, const void* w1_, void* ws_, const void* w2_, const void* wo_, const void* wphi_,
                      const void* b1_, const void* b2_, void* out_) {
    __shared__ __attribute__((aligned(16))) char smem[163840];
    const Lds L{(LAS char*)smem};
    u32x16 ka; u32x4 kb;
    {
        const u64 kp = (u64)__builtin_amdgcn_kernarg_segment_ptr();
        asm volatile("s_load_dwordx16 %0, %2, 0\n\ts_load_dwordx4 %1, %2, 64" : "=&s"(ka), "=&s"(kb) : "s"(kp));
    }
    const u32 b = blockIdx.x, xcd = b & 7, bi = b >> 3;
    const u32 l = threadIdx.x & 63, w = __builtin_amdgcn_readfirstlane(threadIdx.x >> 6);
    {
        u64 pc; u32 len;
        asm volatile("s_getpc_b64 %0\n.Lpf_here:\n\ts_mov_b32 %1, .Lpf_end-.Lpf_here" : "=s"(pc), "=s"(len));
        const u32x4 sc = mk_srd((u32)pc, (u32)(pc >> 32), len);
        asm volatile("s_mov_b32 m0, %0\n\ts_nop 3\n\tbuffer_load_dword off, %1, %2 lds" :: "n"(L_ZERO), "s"(sc), "s"((w * 32 + bi) * 128) : "memory");
    }
    const u32 rt = (xcd & 1) * 8 + (bi & 7), isx = (bi >> 3) & 1, ci = (xcd >> 1) * 2 + (bi >> 4);
    const u32 k0 = rt & 7;
    const u32 rb = xcd * 4 + (bi & 3), cb = bi >> 2, prt = rb >> 1;
    const u32 tg = w & 3, hp = w >> 2, r = l & 31, h = l >> 5;
    const u32 wrow = isx ? 3072 : 2752, wrow3 = 3072 + 2752 - wrow;
    const u32 l3 = l >> 3, csw = 16 * ((l & 7) ^ (4 * ((l >> 4) & 1)));
    u32 vx0 = (rt * 128 + 16 * w + l3) * 1024 + 16 * ((l & 7) ^ (l >> 4)), vx1 = (vx0 ^ 64) + 8192;
    u32 vw = (8 * w + (l3 & 3)) * wrow + (isx ? 1024 + ci * 128 : ci * 256) + h * (isx ? 1024 : 128) + csw;
    u32 l16 = l * 16;
    const u32 srow = rt * 128 + (2 * ci + isx) * 8 + w;
    u32 w1k = w * 1024, w2k = w * 2048, k0x = k0 * 128, wh = wrow / 2, w4 = wrow * 4;
    asm volatile("" : "+s"(w1k), "+s"(w2k), "+s"(k0x), "+s"(wh), "+s"(w4));
    PIN(vx0); PIN(vx1); PIN(vw); PIN(l16);
    asm volatile("s_waitcnt lgkmcnt(0)" : "+s"(ka), "+s"(kb));
    u64 epoch;
    asm volatile("s_load_dwordx2 %0, %1, 0" : "=s"(epoch) : "s"((u64)ka[6] | ((u64)ka[7] << 32)));
    const u32x4 srdx = mk_srd(ka[0], ka[1], 2048 * 1024);
    const u32x4 srdw = mk_srd(isx ? ka[2] : ka[4], isx ? ka[3] : ka[5], 512 * wrow);
    const u32x4 srdw3 = mk_srd(isx ? ka[4] : ka[2], isx ? ka[5] : ka[3], 512 * wrow3);
    const u32x4 srdws = mk_srd(ka[6], ka[7], WS_END);
    const u32x4 srd2 = mk_srd(ka[8], ka[9], 1376 * 1024), srdo = mk_srd(ka[10], ka[11], 512 * 1024);
    const rsrc_t rws = __builtin_amdgcn_make_buffer_rsrc((void*)((u64)ka[6] | ((u64)ka[7] << 32)), 0, WS_END, 0x00020000);
    u32 vw3, v2, sto, kx, kw, f32k, f8k, t3[3];
    u32x4 row, st, raw[15], hf[22], gt[8], of[8], xr, br;
    u32 hofs[2], go, oo, outo, colo, flo[2], hso;
    u64 fl;
    const u32 kq = 2 * (tg & 1) + hp;
    auto tail2 = [&](int c) { return (32 + 6 * (c & 1) - 5 * hp + t3[(c >> 1) - 8]) * 32768; };
    auto op = [&](auto K, auto A) {
        constexpr int k = K, a = A;
        if constexpr (k == T) {
            constexpr u32 s = ((a >> 2) & 3) * 16384 + (a & 1) * 1024;
            if constexpr ((a & 3) == 0) kx = dma16w<s, (a >> 2) * 128, 0x380>(w2k, vx0, srdx, k0x);
            else if constexpr ((a & 3) == 1) dma16<s>(w2k, vx1, srdx, kx);
            else if constexpr ((a & 3) == 2) kw = dma16m<L_W + s>(w2k, vw, srdw, kx, wh);
            else dma16a<L_W + s>(w2k, vw, srdw, kw, w4);
        } else if constexpr (k == ROW) ld16(row, l16, srdx, srow * 1024);
        else if constexpr (k == TCH) {
            const u32 tl = seq(w, 0, ka[8], seq(w, 1, ka[10], seq(w, 2, ka[2], ka[4]))), th = seq(w, 0, ka[9], seq(w, 1, ka[11], seq(w, 2, ka[3], ka[5])));
            const u32 rs = seq(w, 2, 3072, seq(w, 3, 2752, 1024)), bs = seq(w, 0, 43 * 128, w < 4 ? 2 * rs : 0);
            dma4(L_ZERO + 256, l3 * rs + (l & 7) * 128, mk_srd(tl, th, (b + 1) * bs), b * bs + seq(w, 3, 1728, 0));
        } else if constexpr (k == SWL) dma4(L_SW + w * 256, l * 4, mk_srd(ka[12], ka[13], 16384), ci * 2048 + w * 256);
        else if constexpr (k == B1L) dma4(L_B1, l * 4, mk_srd(ka[14], ka[15], 2752), isx ? 2048 + ci * 128 : ci * 256);
        else if constexpr (k == SLD) ld16c(st, sto, srdws, 0);
        else if constexpr (k == W3) dma16m<a * 8192>(w1k, vw3, srdw3, wrap<a * 128, 0x380>(k0x), wrow3 / 2);
        else if constexpr (k == CELL && a < 16) dma16w<a * 8192, (((a >> 1) + 4) & 7) * 32768 + (a & 1) * 0x80000, 0xb8000>(w1k, v2, srd2, f32k);
        else if constexpr (k == CELL) dma16<a * 8192>(w1k, v2, srd2, tail2(a));
        else if constexpr (k == ECELL) dma16<a * 8192>(w1k, v2, srd2, isx ? OOB : tail2(a));
        else if constexpr (k == RAW && a < 3) ld16(raw[a], v2, srd2, tail2(19 + a));
        else if constexpr (k == RAW && a < 11) ld16(raw[a], v2, srdo, wrap<(a - 3) * 32768, 0x38000>(f32k));
        else if constexpr (k == RAW) ld16(raw[a], v2, srd2, wrap<((a + 1) >> 1) * 32768 + ((a + 1) & 1) * 0x80000, 0xb8000>(f32k));
        else if constexpr (k == GA) ld16c(gt[a], go, srdws, a * REC);
        else if constexpr (k == HL) {
            if constexpr (a < 16 && !(a & 1)) hso = wrap<(a >> 1) * 8192, 0xe000>(f8k);
            else if constexpr (!(a & 1)) { const u32 u = 3 * kq + t3[(a >> 1) - 8]; hso = u == 11 ? OOB : (32 + u - 8 * kq) * 8192; }
            ld16c(hf[a], hofs[a & 1], srdws, hso);
        } else if constexpr (k == FL) ld8c(fl, flo[a], srdws);
        else if constexpr (k == XR) ld16(xr, outo, srdx, 0);
        else if constexpr (k == BR) ld16(br, colo, mk_srd(kb[0], kb[1], 1024), 0);
        else if constexpr (k == OL) ld16c(of[a], oo, srdws, wrap<a * 8192, 0xe000>(f8k));
    };
    auto ops = [&](auto XX, auto S) {
        constexpr bool X = XX;
        constexpr int s = S;
        rep<0, -1 - nth<X>(s, -1)>([&](auto I) { constexpr Op o = opat<X>(nth<X>(s, I)); op(IC<o.kind>{}, IC<o.arg>{}); });
    };
    constexpr std::true_type XT{}; constexpr std::false_type YT{};
    ops(YT, IC<-3>{});
    ops(YT, IC<-2>{});
    asm volatile("s_barrier");
    ops(YT, IC<-1>{});
    const u32 q = (l & 15) >> 2, p = l & 3, blk = (l >> 4) & 1, ps = ((p & 1) << 1) | (p >> 1), g = l >> 4, tk = l & 15;
    const u32 rot = tg ^ (2 * hp), inner = q * 128 + 64 * (hp ^ (q >> 1)) + 32 * blk + 8 * ps;
    u32 xa[4], wa[4];
#pragma unroll
    for (int i = 0; i < 4; ++i) {
        const u32 s = i ^ rot;
        xa[i] = (32 * tg + r) * 128 + 16 * ((2 * s + h) ^ ((r >> 1) & 7));
        wa[i] = L_W + (4 * s + 2 * h) * 1024 + inner;
        PIN(xa[i]); PIN(wa[i]);
    }
    {
        const u32 c3 = (isx ? 2048 : 0) + ci * 128 + csw;
        vw3 = c3 < wrow3 ? (8 * w + l3) * wrow3 + c3 : OOB;
        v2 = (256 * hp + 8 * tg + l3) * 1024 + cb * 128 + 16 * ((l & 7) ^ (2 * (blk | ((w & 1) << 1))));
        sto = WS_ST + (rt * 128 + 32 * tg + r) * 16;
        PIN(vw3); PIN(v2); PIN(sto);
        const u32 f8 = (cb + 2 * (bi & 3)) & 7;
        f32k = f8 * 32768; f8k = f8 * 8192;
        t3[0] = f8 - 3 * ((f8 * 11) >> 5); t3[1] = t3[0] == 2 ? 0 : t3[0] + 1; t3[2] = 3 - t3[0] - t3[1];
        asm volatile("" : "+s"(f32k), "+s"(f8k), "+s"(t3[0]), "+s"(t3[1]), "+s"(t3[2]));
    }
    const u32 hm = (l >> 2) & 1;
    u32 int_a = 0x3f803f80u & (hm - 1), int_b = 0x3f803f80u & -hm, sidx = (l & 1) ? 0xeeee : 0x4444;
    u32x4 selu;
    { const u32 m = (((l >> 3) ^ h) & 1) - 1; selu = u32x4{int_a & m, int_a & m, int_b & m, int_b & m}; }
    u32 hmask = h - 1, csa = L_CS + (hp * 32 + r) * 16;
    u32 b1a = L_B1 + (hp * 32 + (l & 16) + 4 * ((hm << 1) | ((l >> 3) & 1)) + p) * 2;
    PIN(selu); PIN(sidx); PIN(hmask); PIN(csa); PIN(b1a);
    asm volatile("s_waitcnt lgkmcnt(0)" : "+s"(epoch));
    const u64 tag = epoch + 1;
    auto rowsums = [&]() {
        waitv<after<false>(ROW, 0, ST_AT + 1) - 1>(row);
        u32 ones = 0x3f803f80u;
        asm("" : "+s"(ones));
        float s = 0.f, sq = 0.f;
#pragma unroll
        for (int d = 0; d < 4; ++d) {
            const u32 rd = row[d];
            s = __builtin_amdgcn_fdot2_f32_bf16(__builtin_bit_cast(bf16x2, rd), __builtin_bit_cast(bf16x2, ones), s, false);
            sq = __builtin_amdgcn_fdot2_f32_bf16(__builtin_bit_cast(bf16x2, rd), __builtin_bit_cast(bf16x2, rd), sq, false);
        }
        const auto s32 = __builtin_amdgcn_permlane32_swap(__float_as_uint(s), __float_as_uint(sq), false, false);
        float v = __uint_as_float(s32[0]) + __uint_as_float(s32[1]);
        v = dpp_add<0x140>(dpp_add<0x141>(dpp_add<0x4e>(dpp_add<0xb1>(v))));
        const auto s16 = __builtin_amdgcn_permlane16_swap(__float_as_uint(v), __float_as_uint(v), false, false);
        v = __uint_as_float(s16[0]) + __uint_as_float(s16[1]);
        const auto sf = __builtin_amdgcn_permlane32_swap(__float_as_uint(v), __float_as_uint(v), false, false);
        st16c(u32x4{sf[0], sf[1], (u32)tag, (u32)(tag >> 32)}, rws, l == 0 ? WS_ST + srow * 16 : OOB);
    };
    if (ST_AT < 0) rowsums();
    STAMP(1);

    u32x4 xf[32];
    f32x16 acc0 = {}, acc1 = {};
    float rs1, ga, gb, sdv;
    bf16x8 xfix;
    u32 bias2;
    u32x4 wp0, wp1;
    waitv_barrier<after<false>(T, 3, 0)>();
    STAMP(2);
    {
        bf16x8 wc[3][2], wf0, wf1;
        f32x4 cs = {}, cp0, cp1;
        u32 bias0, bias1;
        float mean, var, rs2;
        auto rdx = [&](int m) { xf[m] = L.r128(xa[m & 3] + ((m >> 2) & 3) * 16384); };
        auto rdw = [&](int m, int j) { wc[m % 3][j] = L.tr(wa[m & 3] + ((m >> 2) & 3) * 16384 + 512 * j, 1024); };
        rdx(0); rdw(0, 0); rdw(0, 1); rdx(1); rdw(1, 0); rdw(1, 1);
        ops(YT, IC<0>{});
        rep<0, 32>([&](auto N) {
            constexpr int n = N, t = n >> 2, i = n & 3;
            if constexpr (i == 3 && t < 7) {
                STAMPX(1, 3 + t);
                if constexpr (t == 5) { waitv_barrier<cmin(after<false>(T, 27, 1 + 2 * n), after<false>(SLD, 0, 1 + 2 * n))>(); PIN(st); }
                else waitv_barrier<after<false>(T, 4 * t + 7, 1 + 2 * n)>();
            }
            if constexpr (n == 30) { STAMPX(1, 10); waitv_barrier<after<false>(ECELL, 18, 61)>(); cp0 = L.r128f(csa); cp1 = L.r128f(csa + 1024); }
            SB();
            if constexpr (n == 28) { cs = SMFMA(asfrag(selu), cat(wc[n % 3][0], wc[n % 3][1]), cs, sidx); SB(); }
            acc0 = MFMA32(wc[n % 3][0], asfrag(xf[n]), acc0);
            PIN(acc0);
            SB();
            prio<3 - ((n + 1) & 3)>();
            if constexpr (i == 3 && t < 7) { rdx(n + 1); rdw(n + 1, 0); rdw(n + 1, 1); }
            else if constexpr (i != 2 && n < 30) { rdx(n + 2); rdw(n + 2, 0); }
            ops(YT, IC<1 + 2 * n>{});
            if constexpr (n == 24) {
                mean = __uint_as_float(st[0]) * (1.f / 512.f);
                var = fmaxf(__uint_as_float(st[1]) * (1.f / 512.f) - mean * mean, 0.f);
                rs1 = __builtin_amdgcn_rsqf(var + 1e-5f);
                PIN(rs1); PIN(var); PIN(mean);
            }
            if constexpr (n == 25) { sdv = __builtin_amdgcn_rcpf(rs2); ga = -2.f * 0.7978845608028654f * LOG2E * rs2; gb = ga * 0.044715f * rs2 * rs2; PIN(sdv); PIN(ga); PIN(gb); }
            if constexpr (n == 26) {
                const u32 hs = pk2(-mean, sdv);
                float lm = -mean - __uint_as_float(hs << 16); PIN(lm);
                const u32 ls = pk2(lm, sdv - __uint_as_float(hs & 0xffff0000u));
                u32x4 xu = {(hs & 0xffffu) | (ls << 16), hs, ls >> 16, 0u};
                PIN(xu);
                xfix = asfrag(xu);
            }
            if constexpr (n == 29) L.w32f(csa + h * 1024 + tg * 4, cs[0] + cs[1]);
            if constexpr (n == 31) wf1 = fix_operand(cp1, bias1, hmask);
            SB();
            acc1 = MFMA32(wc[n % 3][1], asfrag(xf[n]), acc1);
            PIN(acc1);
            SB();
            if constexpr (i == 3 && t < 7) { rdx(n + 2); rdw(n + 2, 0); rdw(n + 2, 1); }
            else if constexpr (i != 2 && n < 30) rdw(n + 2, 1);
            ops(YT, IC<2 + 2 * n>{});
            if constexpr (i == 2 && t < 7) cs = SMFMA(asfrag(selu), cat(wc[n % 3][0], wc[n % 3][1]), cs, sidx);
            if constexpr (2 + 2 * n == ST_AT) rowsums();
            if constexpr (n == 22) {
                const u32 b0 = L.r16(b1a), b1 = L.r16(b1a + 128);
                bias0 = b0 & (isx - 1); bias1 = b1 & (isx - 1); bias2 = b0 & -isx;
                PIN(bias0); PIN(bias1); PIN(bias2);
            }
            if constexpr (n == 23) poll16(st, sto, srdws, tag);
            if constexpr (n == 24) { rs2 = rs1 * __builtin_amdgcn_rsqf(var * rs1 * rs1 + 1e-5f); PIN(rs2); }
            if constexpr (n == 30) wf0 = fix_operand(cp0, bias0, hmask);
            SB();
        });
        acc0 = MFMA32(wf0, xfix, acc0);
        acc1 = MFMA32(wf1, xfix, acc1);
        PIN(acc0); PIN(acc1);
        SB();
    }
    STAMP(3); STAMPX(1, 11);
    auto phi_operand = [&]() {
        const u32x4 wpu = __builtin_bit_cast(u32x4, L.tr(L_SW + (2 * hp + (p & 1)) * 512 + (8 * h + q) * 32 + (2 * blk + (p >> 1)) * 8, 128));
        u32 m0 = hm - 1; PIN(m0);
        wp0 = wpu & m0; wp1 = wpu & ~m0;
        PIN(wp0); PIN(wp1);
    };
    auto logits = [&](const f32x16& a) { return MFMA32(asfrag(wp1), asfrag(pack8(a, 1)), MFMA32(asfrag(wp0), asfrag(pack8(a, 0)), f32x16{})); };
    u32 ba[4];
    auto phase_b_addresses = [&](int part) {
        if (part == 0) {
            hofs[0] = WS_H + ((prt * 172 + 32 * kq + g) * 128 + 64 * (rb & 1) + 16 * tg + tk) * 16; hofs[1] = hofs[0] ^ 256;
            PIN(hofs[0]); PIN(hofs[1]);
        } else if (part == 1) {
            const u32 swz = ((q >> 1) | (blk << 1)) ^ (2 * hp);
            ba[0] = 65536 + kq * 4096 + (8 * g + q) * 128 + 32 * swz + 8 * p;
            PIN(ba[0]);
        } else if (part == 2) {
#pragma unroll
            for (int c = 1; c < 4; ++c) { ba[c] = ba[0] ^ (32 * c); PIN(ba[c]); }
        } else if (part == 3) {
            flo[0] = WS_FG + (prt * 8 + ((3 * kq) >> 1) + (l & 1)) * 128; flo[1] = WS_FO + (prt * 8 + 4 * hp + p) * 128;
            PIN(flo[0]); PIN(flo[1]);
        } else if (part == 4) {
            colo = cb * 128 + 64 * hp + 32 * blk + 16 * h; outo = (rb * 64 + 16 * tg + tk) * 1024 + colo;
            PIN(colo); PIN(outo);
        } else if (part == 5) {
            oo = WS_O + ((prt * 64 + 32 * hp + g) * 128 + 64 * (rb & 1) + 16 * tg + tk) * 16;
            PIN(oo);
        }
    };
    auto pass2 = [&](auto XX, auto step, auto sync) {
        u32 wb[4];
#pragma unroll
        for (int i = 0; i < 4; ++i) { wb[i] = (4 * (i ^ rot) + 2 * h) * 512 + inner; PIN(wb[i]); }
        bf16x8 f[3], c0, c1, wf;
        f32x16 acc = {};
        f32x4 cs = {}, cp;
        sync(IC<0>{});
        f[0] = L.tr(wb[0], 512); f[1] = L.tr(wb[1], 512);
        u32x4 sel2 = {int_a, int_a, int_b, int_b};
        PIN(sel2);
        rep<0, 32>([&](auto N) {
            constexpr int n = N;
            if constexpr (n == MIDB || n == 30) sync(N);
            SB();
            acc = MFMA32(f[n % 3], asfrag(xf[n]), acc);
            PIN(acc);
            SB();
            if constexpr (((n + 2) & 3) == 0) prio<3 - (((n + 2) & 15) >> 2)>();
            if constexpr (n + 2 < 32) f[(n + 2) % 3] = L.tr(wb[(n + 2) & 3] + 8192 * ((n + 2) >> 2), 512);
            if constexpr (n >= 2 && n <= 8 && !(n & 1)) { cs = SMFMA(asfrag(sel2), cat(c0, c1), cs, sidx); PIN(cs); SB(); }
            if constexpr (n <= 6 && !(n & 1)) { c0 = L.tr(wb[0] + 8192 * n, 512); c1 = L.tr(wb[0] + 8192 * (n + 1), 512); }
            if constexpr (n == 11) L.w32f(csa + 2048 + tg * 4, cs[0] + cs[1]);
            if constexpr (n == MIDB) cp = L.r128f(csa + 2048);
            if constexpr (n == MIDB + 2) wf = fix_operand(cp, XX ? bias2 : 0, hmask);
            if constexpr (n == MIDB + 4) { acc = MFMA32(wf, xfix, acc); PIN(acc); }
            ops(XX, IC<200 + n>{});
            step(N);
            SB();
        });
        return acc;
    };
    auto gelu2 = [&](float a, float c) {
        const f32x2 gap = {ga, ga}, gbp = {gb, gb}, sdp = {sdv, sdv}, v = {a, c};
        const f32x2 z = v * __builtin_elementwise_fma(v * v, gbp, gap);
        const f32x2 e = {__builtin_amdgcn_exp2f(z[0]), __builtin_amdgcn_exp2f(z[1])};
        const f32x2 d = __builtin_elementwise_fma(e, sdp, sdp);
        const f32x2 y = v * f32x2{__builtin_amdgcn_rcpf(d[0]), __builtin_amdgcn_rcpf(d[1])};
        return pk2(y[0], y[1]);
    };

    auto phase_b = [&](auto XX) {
        constexpr bool X = XX;
        constexpr int SG = X ? X_FG : Y_FG, SO = X ? X_FO : Y_FO;
        f32x4 am[2][4], as[4];
#pragma unroll
        for (int c = 0; c < 4; ++c) { am[0][c] = f32x4{}; am[1][c] = f32x4{}; as[c] = f32x4{}; }
        u32 wl = w1k + l16, wl2 = wl + 65536, wl3 = wl + L_SCR, bb[4];
        s16x4 f[2][8];
#pragma unroll
        for (int m = 0; m < 8; ++m) f[0][m] = L.trh(ba[m >> 1] + 512 * (m & 1));
        PIN(wl); PIN(wl2); PIN(wl3);
        rep<0, 11>([&](auto S) {
            constexpr int s = S, n = s + 1, at = 400 + 8 * s;
            constexpr int wh = after<X>(HL, 2 * s + 1, at);
            if constexpr (s == 4) {
                waitv_barrier<X ? cmin(wh, after<X>(CELL, 7, at)) : wh>();
                flag<1>(w, tag, srdws, (X ? WS_FG : WS_FO) + (rt * 8 + ci) * 128);
                rep<0, 11>([&](auto J) { PIN(raw[J]); });
                STAMP(9);
            } else if constexpr (X && s == 7) waitv_barrier<cmin(wh, after<X>(CELL, 18, at))>();
            else if constexpr (s == 8) waitv_barrier<wh>();
            if constexpr (s == SG) poll<after<X>(FL, 0, at)>(fl, flo[0], srdws, tag);
            if constexpr (s == SO) poll<after<X>(FL, 1, at)>(fl, flo[1], srdws, tag);
            waitv<wh>(hf[2 * s], hf[2 * s + 1]);
            rep<0, 8>([&](auto M) {
                constexpr int m = M, c = m >> 1;
                SB();
                am[m & 1][c] = MFMA16(Lds::join(f[s & 1][2 * c], f[s & 1][2 * c + 1]), asfrag(hf[2 * s + (m & 1)]), am[m & 1][c]);
                PIN(am[m & 1][c]);
                SB();
                if constexpr (m == 0) prio<3 - (s & 3)>();
                if constexpr (n < 11) {
                    if constexpr (!(m & 1) && (n == 4 || n == 8)) { if constexpr (n == 4) { bb[c] = ba[c] - 65536; PIN(bb[c]); } else { ba[c] += 65536; PIN(ba[c]); } }
                    f[n & 1][m] = L.trh((n < 4 || n == 8 || n == 9 ? ba[c] : bb[c]) + (n == 10 ? 0 : (n & 3) * 16384) + 512 * (m & 1));
                }
                ops(XX, IC<at + m>{});
                constexpr int j = 3 * (s - 4) + (m - 2) / 2;
                if constexpr (s >= 4 && m >= 2 && (m & 1) && j < 11) L.w128(j == 0 ? wl3 : (j < 3 ? wl + (j - 1) * 8192 : wl2 + (j - 3) * 8192), raw[j]);
            });
        });
        SB();
        STAMP(10);
        if constexpr (SO == 11) poll<after<X>(FL, 1, 500)>(fl, flo[1], srdws, tag);
        ops(XX, IC<500>{});
        STAMP(11);
        u32 xo = L_XM + w2k + l16;
        PIN(xo);
#pragma unroll
        for (int k = 1; k < 4; ++k) {
            L.w128f(xo + (k - 1) * 16384, am[k >> 1][2 * (k & 1)]);
            L.w128f(xo + (k - 1) * 16384 + 1024, am[k >> 1][2 * (k & 1) + 1]);
        }
        bf16x8 wf[3][4];
#pragma unroll
        for (int c = 0; c < 4; ++c) { bb[c] += 65536 - (tg & 1) * 8192; PIN(bb[c]); }
        waitv<after<X>(BR, 0, 501)>(xr, br);
        const auto b0 = __builtin_amdgcn_permlane16_swap(br[0], br[2], false, false), b1 = __builtin_amdgcn_permlane16_swap(br[1], br[3], false, false);
        const auto x0 = __builtin_amdgcn_permlane16_swap(xr[0], xr[2], false, false), x1 = __builtin_amdgcn_permlane16_swap(xr[1], xr[3], false, false);
        u32 xres[4] = {x0[0], x1[0], x0[1], x1[1]}, xi[3] = {xo ^ 8192, xo ^ 2048, xo ^ 10240};
        f32x4 mm[2];
#pragma unroll
        for (int j = 0; j < 2; ++j) {
            mm[j] = am[0][j] + f32x4{__uint_as_float(b0[j] << 16), __uint_as_float(b0[j] & 0xffff0000u), __uint_as_float(b1[j] << 16), __uint_as_float(b1[j] & 0xffff0000u)};
            PIN(mm[j]); PIN(xres[2 * j]); PIN(xres[2 * j + 1]);
        }
        PIN(xi[0]); PIN(xi[1]); PIN(xi[2]);
        BARRIER();
        f32x4 gate[2];
        {
            f32x4 pm[2][3];
#pragma unroll
            for (int j = 0; j < 2; ++j)
#pragma unroll
                for (int k = 0; k < 3; ++k) pm[j][k] = L.r128f(xi[k] + k * 16384 + j * 1024);
            SB();
#pragma unroll
            for (int j = 0; j < 2; ++j) {
#pragma unroll
                for (int c = 0; c < 4; ++c) wf[j][c] = L.tr(bb[c] + j * 8192, 512);
                const f32x4 m = (mm[j] + pm[j][0]) + (pm[j][1] + pm[j][2]), z = m * -LOG2E;
                const f32x4 d = f32x4{__builtin_amdgcn_exp2f(z[0]), __builtin_amdgcn_exp2f(z[1]), __builtin_amdgcn_exp2f(z[2]), __builtin_amdgcn_exp2f(z[3])} + 1.f;
                gate[j] = m * f32x4{__builtin_amdgcn_rcpf(d[0]), __builtin_amdgcn_rcpf(d[1]), __builtin_amdgcn_rcpf(d[2]), __builtin_amdgcn_rcpf(d[3])};
                PIN(gate[j]);
                SB();
            }
        }
        u32 xs = L_XS + w2k + l16;
        PIN(xs);
        rep<0, 8>([&](auto J) {
            constexpr int j = J;
            waitv<after<X>(OL, j, 501)>(of[j]);
            rep<0, 4>([&](auto C) {
                constexpr int c = j == 7 ? (C + 2) & 3 : C;
                SB();
                as[c] = MFMA16(wf[j % 3][c], asfrag(of[j]), as[c]);
                PIN(as[c]);
                SB();
                if constexpr (C == 0 && !(j & 1)) prio<3 - (j >> 1)>();
                if constexpr (j < 6) wf[(j + 2) % 3][c] = L.tr(bb[c] + (j + 2) * 8192, 512);
            });
        });
        L.w128f(xs, as[2]);
        L.w128f(xs + 1024, as[3]);
        xs ^= 8192;
        f32x4 y0[2];
#pragma unroll
        for (int j = 0; j < 2; ++j) {
            y0[j] = __builtin_elementwise_fma(as[j], gate[j], f32x4{__uint_as_float(xres[2 * j] << 16), __uint_as_float(xres[2 * j] & 0xffff0000u),
                                                                     __uint_as_float(xres[2 * j + 1] << 16), __uint_as_float(xres[2 * j + 1] & 0xffff0000u)});
            PIN(y0[j]);
        }
        BARRIER();
        STAMP(12);
        {
            u32 y[4];
#pragma unroll
            for (int j = 0; j < 2; ++j) {
                const f32x4 v = __builtin_elementwise_fma(L.r128f(xs + j * 1024), gate[j], y0[j]);
                y[2 * j] = pk2(v[0], v[1]); y[2 * j + 1] = pk2(v[2], v[3]);
            }
            const auto s0 = __builtin_amdgcn_permlane16_swap(y[0], y[2], false, false), s1 = __builtin_amdgcn_permlane16_swap(y[1], y[3], false, false);
            const rsrc_t ro = __builtin_amdgcn_make_buffer_rsrc((void*)((u64)kb[2] | ((u64)kb[3] << 32)), 0, 2048 * 1024, 0x00020000);
            __builtin_amdgcn_raw_buffer_store_b128(u32x4{s0[0], s1[0], s0[1], s1[1]}, ro, outo, 0, 0);
        }
    };

    if (isx) {
        {
            phi_operand();
            u32 va = L_V + (32 * tg + r) * 128 + 16 * ((4 * hp + h) ^ (r & 7));
            u32 pa = L_PHI + ((2 * hp + h) * 128 + 32 * tg + r) * 64 + 16 * ((r >> 1) & 3);
            PIN(va); PIN(pa);
            SB();
            const f32x16 pk = logits(acc0);
            acc1 *= rs1;
            L.w128(va, pack8(acc1, 0));
            L.w128(va ^ 32, pack8(acc1, 1));
            f32x2 ep[8], en[8], sp, sn;
            dual_exp(pk, rs1, ep, en, [&](auto I) {
                if constexpr (I == 0) { sp = ep[0]; sn = en[0]; } else { sp += ep[I]; sn += en[I]; }
                if constexpr (I & 1) { SB(); ops(XT, IC<102 + I / 2>{}); }
            });
            const float rp = __builtin_amdgcn_rcpf(sp[0] + sp[1]), rn = __builtin_amdgcn_rcpf(sn[0] + sn[1]);
#pragma unroll
            for (int c = 0; c < 4; ++c) {
                u32x4 o;
#pragma unroll
                for (int e = 0; e < 4; ++e) {
                    const int i = (c & 2) + (e & 1) + 2 * (e & 2);
                    const f32x2 a = (c & 1) ? en[i] * rn : ep[i] * rp;
                    o[e] = pk2(a[0], a[1]);
                }
                L.w128(pa ^ (16 * c), o);
            }
        }
        {
            u32 a0 = L_PHI + (tg * 128 + 4 * g + q) * 64 + 16 * ((p >> 1) ^ (2 * blk + (q >> 1))) + 8 * (p & 1), a1 = a0 ^ 32;
            u32 b0 = L_V + (4 * g + q) * 128 + 16 * ((2 * tg + (p >> 1)) ^ (4 * blk + q)) + 8 * (p & 1);
            u32 ko = WS_PKV + (ci * 16 + rt) * REC + (hp ? (tk == 0 ? 4096 + tg * 64 + l : OOB) : tg * 1024 + l16);
            PIN(a0); PIN(a1); PIN(b0); PIN(ko);
            waitv_barrier<after<true>(W3, 7, 150)>();
            STAMP(4);
            bf16x8 af[4][2], vf[4];
#pragma unroll
            for (int s = 0; s < 4; ++s) { af[s][0] = L.tr(a0 + s * 2048, 1024); vf[s] = L.tr(b0 + s * 4096, 2048); af[s][1] = L.tr(a1 + s * 2048, 1024); }
            if (hp) {
#pragma unroll
                for (int s = 0; s < 4; ++s) vf[s] = asfrag(u32x4{0x3f803f80u, 0x3f803f80u, 0x3f803f80u, 0x3f803f80u});
            }
            f32x4 k0 = {}, k1 = {};
#pragma unroll
            for (int s = 0; s < 4; ++s) { k0 = MFMA16(af[s][0], vf[s], k0); k1 = MFMA16(af[s][1], vf[s], k1); }
            st16c(u32x4{pk2(k0[0], k0[1]), pk2(k0[2], k0[3]), pk2(k1[0], k1[1]), pk2(k1[2], k1[3])}, rws, ko);
            SB();
        }
        STAMP(5);
        u32 fo, h2;
        const f32x16 acc2 = pass2(XT, [&](auto N) {
            constexpr int n = N;
            if constexpr (n == 9) { fo = WS_FH + (prt * 8 + (l & 7)) * 128; PIN(fo); }
            if constexpr (n == 10) { h2 = (2 * ci + hp < 11) ? WS_H + ((rt * 172 + 128 + 8 * ci + 4 * hp + h) * 128 + 32 * tg + r) * 16 : OOB; PIN(h2); }
            if constexpr (n >= 20 && n < 26) phase_b_addresses(n - 20);
            if constexpr (n == 27) ld8c_w0(fl, w, fo, srdws);
        }, [&](auto N) {
            if constexpr (N == MIDB) {
                waitv_barrier<after<true>(KST, 0, 200 + N)>();
                flag<1>(w, tag, srdws, WS_FP + (ci * 16 + rt) * 128);
                STAMP(6);
            } else if constexpr (N == 30) {
                poll_w0<1>(fl, w, fo, srdws, tag);
                BARRIER();
            }
        });
        STAMP(7);
        u32x4 o[2];
        rep<0, 8>([&](auto I) {
            ops(XT, IC<300 + I>{});
            o[I >> 2][I & 3] = gelu2(acc2[2 * I], acc2[2 * I + 1]);
            if constexpr (I & 1) SB();
        });
        st16c(o[0], rws, h2);
        st16c(o[1], rws, h2 + 4096);
        SB();
        ops(XT, IC<310>{});
        waitv_barrier<after<true>(CELL, 3, 311)>();
        STAMP(8);
        phase_b(XT);
#pragma unroll
        for (int n = 0; n < 32; ++n) asm volatile("" : "=v"(xf[n]));
        asm volatile("" : "=v"(acc0), "=v"(acc1), "=v"(xfix));
    }
    u32 isx2 = isx;
    asm volatile("" : "+s"(isx2));
    if (!isx2) {
        {
            u32 ho = WS_H + ((rt * 172 + 16 * ci + 4 * hp + h) * 128 + 32 * tg + r) * 16;
            PIN(ho);
            if (w == 2) { const u32 v = (l >> 1) == 1 ? 0x3f803f80u : 0u; L.w128(L_ZERO + l16, u32x4{v, v, v, v}); }
            u32x4 o;
            rep<0, 16>([&](auto I) {
                constexpr int i = I;
                ops(YT, IC<100 + i>{});
                o[i & 3] = i < 8 ? gelu2(acc0[2 * (i & 7)], acc0[2 * (i & 7) + 1]) : gelu2(acc1[2 * (i & 7)], acc1[2 * (i & 7) + 1]);
                if constexpr ((i & 3) == 3) st16c(o, rws, ho + (i >> 3) * 16384 + ((i >> 2) & 1) * 4096);
                if constexpr (i & 1) SB();
            });
        }
        STAMP(4);
        u32 fo, kvw, fa, dz0, dz1, os;
        u32x4 one;
        const f32x16 acc2 = pass2(YT, [&](auto N) {
            constexpr int n = N;
            if constexpr (n == 9) { fo = blk ? WS_FH + (prt * 8 + (l & 7)) * 128 : WS_FP + (ci * 16 + tk) * 128; PIN(fo); }
            if constexpr (n == 10) { go = WS_PKV + (ci * 16 + 8 * h) * REC + (32 * w + r) * 16; PIN(go); }
            if constexpr (n == 12) phi_operand();
            if constexpr (n == 13) { kvw = L_KV + (32 * w + 16 * h + tk) * 16 + 8 * blk; PIN(kvw); }
            if constexpr (n == 15) { fa = hm == h ? L_KV + (2 * hp + h) * 1024 + (p + 4 * (r >> 3)) * 16 : L_ZERO; PIN(fa); }
            if constexpr (n == 17) {
                const u32 z = L_ZERO + ((p & 2) ? 32 : 128 + (2 * hp + h) * 256 + 32 * (p & 1));
                dz0 = (q == g && !(p & 1)) ? z : L_ZERO; dz1 = (q == g && (p & 1)) ? z : L_ZERO;
                PIN(dz0); PIN(dz1);
            }
            if constexpr (n == 19) {
                const u64 v = (u64)(((l >> 3) ^ (l >> 4)) & 1 ? 0u : 0x3f80u) << (16 * p);
                one = u32x4{(u32)v & (hm - 1), (u32)(v >> 32) & (hm - 1), (u32)v & -hm, (u32)(v >> 32) & -hm};
                PIN(one);
            }
            if constexpr (n >= 20 && n < 26) phase_b_addresses(n - 20);
            if constexpr (n == 26) { ld8c_w0(fl, w, fo, srdws); os = WS_O + ((rt * 64 + 2 * (4 * ci + 2 * hp + h)) * 128 + 32 * tg + r) * 16; PIN(os); }
        }, [&](auto N) {
            if constexpr (N == 0) waitv_barrier<after<false>(W3, 7, 200)>();
            else if constexpr (N == MIDB) {
                waitv_barrier<after<false>(HST, 3, 200 + N)>();
                flag<1>(w, tag, srdws, WS_FH + (rt * 8 + ci) * 128);
                STAMP(5);
            } else {
                poll_w0<0>(fl, w, fo, srdws, tag);
                BARRIER();
                STAMP(6);
                if (w == 7) {
                    const u32 zo = WS_PKV + (ci * 16 + g) * REC + 4096 + tk * 16;
#pragma unroll
                    for (int m = 0; m < 4; ++m) ld16c(of[m], zo, srdws, 4 * m * REC);
                }
            }
        });
        u32x4 pq[4];
        {
            ops(YT, IC<300>{});
            asm volatile("s_waitcnt vmcnt(%4)" : "+v"(raw[11]), "+v"(raw[12]), "+v"(raw[13]), "+v"(raw[14]) : "n"(after<false>(RAW, 14, 301)) : "memory");
            u32 wl = w1k + l16; PIN(wl);
            L.w128(wl + 4 * 8192, raw[11]); L.w128(wl + 5 * 8192, raw[12]);
            SB();
            f32x16 pp = logits(acc2);
            PIN(pp);
            SB();
            ops(YT, IC<301>{});
            L.w128(wl + 6 * 8192, raw[13]); L.w128(wl + 7 * 8192, raw[14]);
            SB();
            f32x2 ep[8], en[8];
            dual_exp(pp, rs1, ep, en, [&](auto) {});
#pragma unroll
            for (int i = 0; i < 8; ++i) { pq[i >> 2][i & 3] = pk2(ep[i][0], ep[i][1]); pq[2 + (i >> 2)][i & 3] = pk2(en[i][0], en[i][1]); }
#pragma unroll
            for (int i = 0; i < 4; ++i) PIN(pq[i]);
            SB();
        }
        STAMP(7);
        {
            f32x4 sm = {};
            rep<0, 8>([&](auto J) {
                waitv<after<false>(GA, J, 310 + J)>(gt[J]);
                sm = MFMA16(asfrag(one), asfrag(gt[J]), sm);
                PIN(sm);
                SB();
                ops(YT, IC<310 + J>{});
                SB();
            });
            if (w == 7) {
                const u64 v = (u64)0x3f80u << (16 * p);
                const u32x4 all = {(u32)v & (hm - 1), (u32)(v >> 32) & (hm - 1), (u32)v & -hm, (u32)(v >> 32) & -hm};
                f32x4 sz = {};
#pragma unroll
                for (int m = 0; m < 4; ++m) { PIN(of[m]); sz = MFMA16(asfrag(all), asfrag(of[m]), sz); }
                L.w64(L_ZERO + 128 + q * 256 + p * 16 + 8 * blk, u32x2{pk2(sz[0], sz[1]), pk2(sz[2], sz[3])});
            }
            L.w64(kvw, u32x2{pk2(sm[0], sm[1]), pk2(sm[2], sm[3])});
        }
        BARRIER();
        {
            u32x4 zf[4], kf[4];
#pragma unroll
            for (int s = 0; s < 4; ++s) zf[s] = L.r128((s < 2 ? dz0 : dz1) + (s & 1) * 16);
#pragma unroll
            for (int s = 0; s < 4; ++s) kf[s] = L.r128(fa + s * 256);
            SB();
            f32x4 dn = {};
#pragma unroll
            for (int s = 0; s < 4; ++s) dn = MFMA16(asfrag(zf[s]), asfrag(pq[s]), dn);
            PIN(dn);
            SB();
            f32x16 np = MFMA32(asfrag(kf[1]), asfrag(pq[1]), MFMA32(asfrag(kf[0]), asfrag(pq[0]), f32x16{}));
            f32x16 nn = MFMA32(asfrag(kf[2]), asfrag(pq[2]), f32x16{});
            PIN(np); PIN(nn);
            SB();
            const float rd = __builtin_amdgcn_rcpf(fmaf(dn[3], dn[0], dn[2] * fmaf(dn[3], 1e-6f, dn[1])));
            float cp = dn[3] * rd; PIN(cp);
            float cn = dn[2] * rd; PIN(cn);
            SB();
            nn = MFMA32(asfrag(kf[3]), asfrag(pq[3]), nn);
            const f32x16 o = __builtin_elementwise_fma(nn, f32x16{} + cn, np * cp);
            st16c(pack8(o, 0), rws, os);
            st16c(pack8(o, 1), rws, os + 2048);
            SB();
            asm volatile("s_nop 0");
            SB();
        }
        STAMP(8);
        phase_b(YT);
    }
    if (b == 0 && w == 0) {
        poll<0>(~tag, WS_FH + l * 128, srdws, tag);
        poll<0>(~tag, WS_FH + (64 + l) * 128, srdws, tag);
        flag<0>(w, tag, srdws, 0);
    }
    asm volatile(".Lpf_end:");
}

extern "C" void kernel_launch(void* const* d_in, const int* in_sizes, int n_in, void* d_out, int out_size, void* d_ws, size_t ws_size,
                              hipStream_t stream) {
    hedgehog_mixer_kernel<<<256, 512, 0, stream>>>(d_in[0], d_in[1], d_in[5], d_ws, d_in[7], d_in[3], d_in[2], d_in[6], d_in[8], d_out);
}
```

```cpp
#define PROF 1
#include <hip/hip_runtime.h>
#include <hip/hip_bf16.h>
#include <type_traits>

typedef unsigned u32;
typedef unsigned long long u64;
typedef __bf16 bf16x2 __attribute__((ext_vector_type(2)));
typedef __bf16 bf16x8 __attribute__((ext_vector_type(8)));
typedef __bf16 bf16x16 __attribute__((ext_vector_type(16)));
typedef short s16x4 __attribute__((ext_vector_type(4)));
typedef float f32x2 __attribute__((ext_vector_type(2)));
typedef float f32x4 __attribute__((ext_vector_type(4)));
typedef float f32x16 __attribute__((ext_vector_type(16)));
typedef u32 u32x2 __attribute__((ext_vector_type(2)));
typedef u32 u32x4 __attribute__((ext_vector_type(4)));
typedef u32 u32x16 __attribute__((ext_vector_type(16)));
#define LAS __attribute__((address_space(3)))
#define DI __device__ __forceinline__
#define SB() __builtin_amdgcn_sched_barrier(0)
template <int N> using IC = std::integral_constant<int, N>;
template <int A, int B, class F> DI void rep(F f) { if constexpr (A < B) { f(IC<A>{}); rep<A + 1, B>(f); } }

#define PROFW 0
#define ROTMODE 8
#define RPOS 2
#define PRIO 1
#define MIDB 14

constexpr u32 WS_FP = 4096;
constexpr u32 WS_FH = WS_FP + 128 * 128;
constexpr u32 WS_FG = WS_FH + 128 * 128;
constexpr u32 WS_FO = WS_FG + 128 * 128;
constexpr u32 WS_ST = WS_FO + 128 * 128;
constexpr u32 REC = 4352;
constexpr u32 WS_PKV = WS_ST + 2048 * 16;
constexpr u32 WS_O = WS_PKV + 128 * REC;
constexpr u32 WS_H = WS_O + 16 * 64 * 2048;
constexpr u32 WS_END = WS_H + 16 * 172 * 2048;
constexpr u32 WS_PROF = 16u << 20;
constexpr u32 OOB = 0x7ff00000u;
static_assert(WS_O % 2048 == 0, "");

constexpr u32 L_W = 65536;
constexpr u32 L_PHI = 13 * 8192;
constexpr u32 L_V = 17 * 8192;
constexpr u32 L_SCR = 19 * 8192;
constexpr u32 L_B1 = L_SCR;
constexpr u32 L_CS = L_SCR + 2048;
constexpr u32 L_SW = L_SCR + 5120;
constexpr u32 L_ZERO = L_SCR + 7168;
constexpr u32 L_KV = L_SCR;
constexpr u32 L_XM = 2 * 8192;
constexpr u32 L_XS = 16 * 8192;

constexpr float LOG2E = 1.4426950408889634f;

enum { T, ROW, TCH, SWL, B1L, SST, SLD, W3, CELL, ECELL, RAW, HST, KST, GA, HL, OST, FL, XR, BR, OL };
struct Op { int at, kind, arg; };
#define TILE(t, a, b, c, d) {a, T, 4 * t}, {b, T, 4 * t + 1}, {c, T, 4 * t + 2}, {d, T, 4 * t + 3}
constexpr int ST_AT = RPOS == 0 ? -1 : (RPOS == 1 ? 6 : 14);
#define COMMON_OPS \
    {RPOS == 0 ? -3 : (RPOS == 1 ? -2 : -1), ROW, 0}, {ST_AT, SST, 0}, \
    TILE(0, -2, -2, -2, -2), TILE(1, -1, -1, -1, -1), TILE(2, 0, 2, 4, 5), TILE(3, 8, 9, 10, 11), TILE(4, 12, 13, 13, 14), \
    {11, TCH, 0}, {11, SWL, 0}, {11, B1L, 0}, \
    TILE(5, 16, 18, 20, 21), TILE(6, 24, 26, 28, 29), {30, ECELL, 16}, {31, SLD, 0}, TILE(7, 32, 34, 36, 37), {38, ECELL, 17}, \
    {40, W3, 0}, {42, W3, 1}, {44, CELL, 8}, {45, CELL, 9}, {46, ECELL, 18}, {48, W3, 2}, {50, W3, 3}, {52, CELL, 10}, {53, CELL, 11}, \
    {56, W3, 4}, {58, W3, 5}, {60, CELL, 12}, {61, W3, 6}, {63, W3, 7}
#define B1_H(s, j) {400 + 8 * (s) + 1, HL, j}, {400 + 8 * (s) + 5, HL, (j) + 1}
constexpr Op YOPS[] = { COMMON_OPS,
    {100, CELL, 13}, {101, CELL, 14}, {102, CELL, 15}, {103, HST, 0}, {105, RAW, 0}, {106, RAW, 1}, {107, HST, 1}, {109, RAW, 2}, {110, RAW, 3},
    {111, HST, 2}, {113, RAW, 4}, {115, HST, 3},
    {201, RAW, 5}, {203, RAW, 6}, {205, RAW, 7}, {207, RAW, 8}, {209, RAW, 9}, {211, RAW, 10}, {213, RAW, 11},
    {215, RAW, 12}, {217, RAW, 13}, {219, RAW, 14}, {220, CELL, 0}, {222, CELL, 1}, {224, CELL, 2}, {226, CELL, 3},
    {230, GA, 0}, {230, GA, 1}, {231, GA, 2}, {231, GA, 3}, {300, GA, 4}, {300, GA, 5}, {301, GA, 6}, {301, GA, 7},
    {310, HL, 0}, {311, HL, 1}, {312, HL, 2}, {313, HL, 3}, {314, HL, 4}, {315, HL, 5}, {316, HL, 6}, {317, HL, 7},
    {330, OST, 0}, {330, OST, 1},
    B1_H(0, 8), B1_H(1, 10), {415, FL, 0}, B1_H(2, 12), B1_H(3, 14), B1_H(4, 16), B1_H(5, 18), B1_H(6, 20), {455, FL, 1},
    {457, XR, 0}, {461, BR, 0},
    {472, OL, 0}, {474, OL, 1}, {476, OL, 2}, {478, OL, 3}, {480, OL, 4}, {482, OL, 5}, {484, OL, 6}, {486, OL, 7} };
constexpr int Y_FG = 4, Y_FO = 9;
constexpr Op XOPS[] = { COMMON_OPS,
    {102, RAW, 0}, {103, RAW, 1}, {104, RAW, 2}, {105, RAW, 3}, {150, KST, 0},
    {203, RAW, 4}, {205, RAW, 5}, {207, RAW, 6}, {209, RAW, 7}, {211, RAW, 8}, {213, RAW, 9},
    {215, CELL, 13}, {217, CELL, 14}, {219, CELL, 15}, {221, CELL, 0}, {223, CELL, 1}, {225, CELL, 2}, {227, CELL, 3}, {229, RAW, 10},
    {230, HL, 0}, {231, HL, 1}, {300, HL, 2}, {301, HL, 3}, {302, HL, 4}, {303, HL, 5}, {304, HL, 6}, {305, HL, 7}, {307, HST, 0}, {307, HST, 1},
    {310, CELL, 4}, {310, CELL, 5}, {403, CELL, 6}, {407, CELL, 7},
    B1_H(0, 8), B1_H(1, 10), B1_H(2, 12), B1_H(3, 14), {433, CELL, 16}, {437, CELL, 17}, {441, CELL, 18}, {445, XR, 0}, {447, BR, 0}, {451, FL, 0},
    {456, HL, 16}, {457, HL, 17}, {458, HL, 18}, {459, HL, 19}, {461, HL, 20}, {463, HL, 21}, {479, FL, 1},
    {500, OL, 0}, {500, OL, 1}, {500, OL, 2}, {500, OL, 3}, {500, OL, 4}, {500, OL, 5}, {500, OL, 6}, {500, OL, 7} };
constexpr int X_FG = 7, X_FO = 11;
template <bool X> constexpr int nops() { return X ? sizeof(XOPS) / sizeof(Op) : sizeof(YOPS) / sizeof(Op); }
template <bool X> constexpr Op opat(int i) { return X ? XOPS[i] : YOPS[i]; }
constexpr bool is_store(int k) { return k == SST || k == HST || k == KST || k == OST; }
template <bool X> constexpr long long okey(int i) { return (opat<X>(i).at * 4LL + (is_store(opat<X>(i).kind) ? 2 : (opat<X>(i).kind == ROW ? 1 : 0))) * 1024 + i; }
template <bool X> constexpr int after(int kind, int arg, int slot) {
    int me = -1, c = 0;
    for (int i = 0; i < nops<X>(); ++i) if (opat<X>(i).kind == kind && opat<X>(i).arg == arg) me = i;
    if (me < 0 || opat<X>(me).at >= slot) return -1000;
    for (int i = 0; i < nops<X>(); ++i) if (okey<X>(i) > okey<X>(me) && opat<X>(i).at < slot) ++c;
    return c;
}
constexpr int cmin(int a, int b) { return a < b ? a : b; }
template <bool X> constexpr int nth(int slot, int j) {
    int c = 0;
    for (int p = 0; p < 2; ++p)
        for (int i = 0; i < nops<X>(); ++i)
            if (opat<X>(i).at == slot && !is_store(opat<X>(i).kind) && (opat<X>(i).kind == ROW) == p && c++ == j) return i;
    return -1 - c;
}

DI u32x4 mk_srd(u32 lo, u32 hi, u32 bytes) { return u32x4{lo, hi & 0xffffu, bytes, 0x00020000u}; }
DI u32 seq(u32 x, u32 c, u32 a, u32 b) {
    u32 r;
    asm("s_cmp_eq_u32 %1, %2\n\ts_cselect_b32 %0, %3, %4" : "=s"(r) : "s"(x), "n"(c), "s"(a), "s"(b) : "scc");
    return r;
}
template <u32 A, u32 M> DI u32 wrap(u32 f) {
    u32 r;
    asm volatile("s_add_u32 %0, %1, %2\n\ts_and_b32 %0, %0, %3" : "=s"(r) : "s"(f), "n"(A), "n"(M) : "scc");
    return r;
}
template <u32 I> DI void dma16(u32 lb, u32 voff, u32x4 srd, u32 soff) {
    asm volatile("s_add_u32 m0, %0, %4\n\ts_nop 0\n\tbuffer_load_dwordx4 %1, %2, %3 offen lds"
                 :: "s"(lb), "v"(voff), "s"(srd), "s"(soff), "n"(I) : "memory", "scc");
}
template <u32 I> DI void dma16a(u32 lb, u32 voff, u32x4 srd, u32 sa, u32 sb) {
    u32 t;
    asm volatile("s_add_u32 m0, %1, %6\n\ts_add_u32 %0, %4, %5\n\tbuffer_load_dwordx4 %2, %3, %0 offen lds"
                 : "=&s"(t) : "s"(lb), "v"(voff), "s"(srd), "s"(sa), "s"(sb), "n"(I) : "memory", "scc");
}
template <u32 I> DI u32 dma16m(u32 lb, u32 voff, u32x4 srd, u32 sa, u32 sb) {
    u32 t;
    asm volatile("s_add_u32 m0, %1, %6\n\ts_mul_i32 %0, %4, %5\n\tbuffer_load_dwordx4 %2, %3, %0 offen lds"
                 : "=&s"(t) : "s"(lb), "v"(voff), "s"(srd), "s"(sa), "s"(sb), "n"(I) : "memory", "scc");
    return t;
}
template <u32 I, u32 A, u32 M> DI u32 dma16w(u32 lb, u32 voff, u32x4 srd, u32 f) {
    u32 t;
    asm volatile("s_add_u32 m0, %1, %5\n\ts_add_u32 %0, %4, %6\n\ts_and_b32 %0, %0, %7\n\tbuffer_load_dwordx4 %2, %3, %0 offen lds"
                 : "=&s"(t) : "s"(lb), "v"(voff), "s"(srd), "s"(f), "n"(I), "n"(A), "n"(M) : "memory", "scc");
    return t;
}
DI void dma4(u32 lds, u32 voff, u32x4 srd, u32 soff) {
    asm volatile("s_mov_b32 m0, %0\n\ts_nop 0\n\tbuffer_load_dword %1, %2, %3 offen lds" :: "s"(lds), "v"(voff), "s"(srd), "s"(soff) : "memory");
}
DI void ld16(u32x4& d, u32 voff, u32x4 srd, u32 soff) {
    asm volatile("buffer_load_dwordx4 %0, %1, %2, %3 offen" : "=v"(d) : "v"(voff), "s"(srd), "s"(soff) : "memory");
}
DI void ld16c(u32x4& d, u32 voff, u32x4 srd, u32 soff) {
    asm volatile("buffer_load_dwordx4 %0, %1, %2, %3 offen sc1" : "=v"(d) : "v"(voff), "s"(srd), "s"(soff) : "memory");
}
DI void ld8c(u64& d, u32 voff, u32x4 srd) {
    asm volatile("buffer_load_dwordx2 %0, %1, %2, 0 offen sc1" : "=v"(d) : "v"(voff), "s"(srd) : "memory");
}
typedef __amdgpu_buffer_rsrc_t rsrc_t;
DI void st16c(u32x4 v, rsrc_t r, u32 voff) { __builtin_amdgcn_raw_buffer_store_b128(v, r, voff, 0, 16); }
#define BARRIER() asm volatile("s_waitcnt lgkmcnt(0)\n\ts_barrier" ::: "memory")
template <int N> DI void waitv_barrier() { static_assert(N >= 0 && N < 64, ""); asm volatile("s_waitcnt vmcnt(%0) lgkmcnt(0)\n\ts_barrier" :: "n"(N) : "memory"); }
template <int N, class V> DI void waitv(V& r) { static_assert(N >= 0 && N < 64, ""); asm volatile("s_waitcnt vmcnt(%1)" : "+v"(r) : "n"(N) : "memory"); }
template <class V> DI void PIN(V& r) { asm volatile("" : "+v"(r)); }
template <int N, class V> DI void waitv(V& r, V& s) { static_assert(N >= 0 && N < 64, ""); asm volatile("s_waitcnt vmcnt(%2)" : "+v"(r), "+v"(s) : "n"(N) : "memory"); }
template <int P> DI void prio() { if (PRIO) asm volatile("s_setprio %0" :: "n"(P)); }
template <int K> DI void flag(u32 w, u64 tag, u32x4 srd, u32 soff) {
    u64 t;
    asm volatile("s_cmp_lg_u32 %1, %5\n\ts_cbranch_scc1 .Lf%=\n\ts_mov_b64 exec, 1\n\tv_mov_b64 %0, %2\n\t"
                 "buffer_store_dwordx2 %0, off, %3, %4 sc1\n\ts_mov_b64 exec, -1\n.Lf%=:"
                 : "=&v"(t) : "s"(w), "s"(tag), "s"(srd), "s"(soff), "n"(K) : "memory", "scc");
}
#define POLL_LOOP(o) "v_cmp_ne_u64 vcc, %[tag], %[f]\n\ts_cbranch_vccz .Ld%=\n\ts_mov_b32 m0, 0x40000\n.Lp%=:\n\ts_sleep 1\n\t" \
    "buffer_load_dwordx2 %[f], %[off], %[srd], 0 offen offset:" #o " sc1\n\ts_sub_u32 m0, m0, 1\n\ts_waitcnt vmcnt(0)\n\ts_cbranch_scc1 .Le%=\n\t" \
    "v_cmp_ne_u64 vcc, %[tag], %[f]\n\ts_cbranch_vccnz .Lp%=\n.Le%=:\n\t"
template <int N> DI void poll(u64 f, u32 off, u32x4 srd, u64 tag) {
    static_assert(N >= 0 && N < 64, "");
    asm volatile("s_waitcnt vmcnt(%[n])\n\t" POLL_LOOP(0) "\n.Ld%=:" : [f] "+v"(f) : [off] "v"(off), [srd] "s"(srd), [tag] "s"(tag), [n] "n"(N) : "memory", "scc", "vcc");
}
DI void ld8c_w0(u64& f, u32 w, u32 off, u32x4 srd) {
    asm volatile("s_cmp_lg_u32 %1, 0\n\ts_cbranch_scc1 .Ld%=\n\tbuffer_load_dwordx2 %0, %2, %3, 0 offen sc1\n.Ld%=:"
                 : "=v"(f) : "s"(w), "v"(off), "s"(srd) : "memory", "scc");
}
template <int N> DI void poll_w0(u64 f, u32 w, u32 off, u32x4 srd, u64 tag) {
    asm volatile("s_cmp_lg_u32 %[w], 0\n\ts_cbranch_scc1 .Ld%=\n\ts_waitcnt vmcnt(%[n])\n\t" POLL_LOOP(0) "\n.Ld%=:"
                 : [f] "+v"(f) : [w] "s"(w), [off] "v"(off), [srd] "s"(srd), [tag] "s"(tag), [n] "n"(N) : "memory", "scc", "vcc");
}
DI void poll16(u32x4& st, u32 off, u32x4 srd, u64 tag) {
    u64 f = (u64)st[2] | ((u64)st[3] << 32);
    asm volatile(POLL_LOOP(8) "buffer_load_dwordx4 %[st], %[off], %[srd], 0 offen sc1\n\ts_waitcnt vmcnt(0)\n.Ld%=:"
                 : [f] "+v"(f), [st] "+v"(st) : [off] "v"(off), [srd] "s"(srd), [tag] "s"(tag) : "memory", "scc", "vcc");
}

DI u32 pk2(float a, float b) {
    f32x2 f = {a, b};
    return __builtin_bit_cast(u32, __builtin_convertvector(f, bf16x2));
}
DI bf16x8 asfrag(u32x4 v) { return __builtin_bit_cast(bf16x8, v); }
DI bf16x16 cat(bf16x8 a, bf16x8 b) { return __builtin_shufflevector(a, b, 0, 1, 2, 3, 4, 5, 6, 7, 8, 9, 10, 11, 12, 13, 14, 15); }
#define MFMA32(a, b, c) __builtin_amdgcn_mfma_f32_32x32x16_bf16((a), (b), (c), 0, 0, 0)
#define MFMA16(a, b, c) __builtin_amdgcn_mfma_f32_16x16x32_bf16((a), (b), (c), 0, 0, 0)
#define SMFMA(a, b, c, i) __builtin_amdgcn_smfmac_f32_16x16x64_bf16((a), (b), (c), (i), 0, 0)

struct Lds {
    LAS char* p;
    DI u32x4 r128(u32 a) const { return *(LAS u32x4*)(p + a); }
    DI f32x4 r128f(u32 a) const { return *(LAS f32x4*)(p + a); }
    DI void w128(u32 a, u32x4 v) const { *(LAS u32x4*)(p + a) = v; }
    DI void w128f(u32 a, f32x4 v) const { *(LAS f32x4*)(p + a) = v; }
    DI void w64(u32 a, u32x2 v) const { *(LAS u32x2*)(p + a) = v; }
    DI void w32f(u32 a, float v) const { *(LAS float*)(p + a) = v; }
    DI u32 r16(u32 a) const { return *(LAS unsigned short*)(p + a); }
    DI s16x4 trh(u32 a) const { return __builtin_amdgcn_ds_read_tr16_b64_v4i16((LAS s16x4*)(p + a)); }
    static DI bf16x8 join(s16x4 lo, s16x4 hi) { return __builtin_bit_cast(bf16x8, __builtin_shufflevector(lo, hi, 0, 1, 2, 3, 4, 5, 6, 7)); }
    DI bf16x8 tr(u32 a, u32 d) const { return join(trh(a), trh(a + d)); }
};

template <class F> DI void dual_exp(const f32x16& p, float sc, f32x2 (&ep)[8], f32x2 (&en)[8], F hook) {
    float mx = p[0], mn = p[0];
#pragma unroll
    for (int i = 1; i < 16; ++i) { mx = __builtin_elementwise_maximum(mx, p[i]); mn = __builtin_elementwise_minimum(mn, p[i]); }
    const float c = sc * LOG2E;
    const f32x2 cp = {c, c}, cn = {-c, -c};
    const float bx = -mx * c, bn = mn * c;
    const f32x2 bxp = {bx, bx}, bnp = {bn, bn};
    rep<0, 8>([&](auto I) {
        constexpr int i = I;
        const f32x2 v = {p[2 * i], p[2 * i + 1]};
        const f32x2 a = __builtin_elementwise_fma(v, cp, bxp), b = __builtin_elementwise_fma(v, cn, bnp);
        ep[i] = f32x2{__builtin_amdgcn_exp2f(a[0]), __builtin_amdgcn_exp2f(a[1])};
        en[i] = f32x2{__builtin_amdgcn_exp2f(b[0]), __builtin_amdgcn_exp2f(b[1])};
        hook(I);
    });
}
DI u32x4 pack8(const f32x16& a, int s) {
    return u32x4{pk2(a[8 * s], a[8 * s + 1]), pk2(a[8 * s + 2], a[8 * s + 3]), pk2(a[8 * s + 4], a[8 * s + 5]), pk2(a[8 * s + 6], a[8 * s + 7])};
}
DI bf16x8 fix_operand(f32x4 cp, u32 bias, u32 hmask) {
    float a = cp[0] + cp[1]; PIN(a);
    a += cp[2]; PIN(a);
    const float c0 = a + cp[3];
    const u32 chh = pk2(c0, c0);
    const u32 cl = pk2(c0 - __uint_as_float(chh << 16), 0.f);
    u32x4 wf = {chh & hmask, (cl | (bias << 16)) & hmask, bias & hmask, 0u};
    PIN(wf);
    return asfrag(wf);
}
template <int C> DI float dpp_add(float v) {
    return v + __int_as_float(__builtin_amdgcn_update_dpp(0, __float_as_int(v), C, 15, 15, true));
}

#define STAMPOP "s_memrealtime"
#define SSET 0
DI void stamp(u64& t) { asm volatile(STAMPOP " %0\n\ts_waitcnt lgkmcnt(0)" : "=s"(t)); }
#define STAMPX(set, i) do { if (set == SSET || i == 0 || i == 13) { SB(); stamp(stamps[i]); SB(); } } while (0)
#define STAMP(i) STAMPX(0, i)

__global__ void __launch_bounds__(512, 2)
hedgehog_mixer_kernel(const void* x_, const void* wqkv_, const void* w1_, void* ws_, const void* w2_, const void* wo_, const void* wphi_,
                      const void* b1_, const void* b2_, void* out_) {
    __shared__ __attribute__((aligned(16))) char smem[163840];
    const Lds L{(LAS char*)smem};
    u64 stamps[16];
#pragma unroll
    for (int i = 0; i < 16; ++i) stamps[i] = 0;
    STAMP(0);
    u32x16 ka; u32x4 kb;
    {
        const u64 kp = (u64)__builtin_amdgcn_kernarg_segment_ptr();
        asm volatile("s_load_dwordx16 %0, %2, 0\n\ts_load_dwordx4 %1, %2, 64" : "=&s"(ka), "=&s"(kb) : "s"(kp));
    }
    const u32 b = blockIdx.x, xcd = b & 7, bi = b >> 3;
    const u32 l = threadIdx.x & 63, w = __builtin_amdgcn_readfirstlane(threadIdx.x >> 6);
    {
        u64 pc; u32 len;
        asm volatile("s_getpc_b64 %0\n.Lpf_here:\n\ts_mov_b32 %1, .Lpf_end-.Lpf_here" : "=s"(pc), "=s"(len));
        const u32x4 sc = mk_srd((u32)pc, (u32)(pc >> 32), len);
        asm volatile("s_mov_b32 m0, %0\n\ts_nop 3\n\tbuffer_load_dword off, %1, %2 lds" :: "n"(L_ZERO), "s"(sc), "s"((w * 32 + bi) * 128) : "memory");
    }
    const u32 rt = (xcd & 1) * 8 + (bi & 7), isx = (bi >> 3) & 1, ci = (xcd >> 1) * 2 + (bi >> 4);
    const u32 k0 = rt & 7;
    const u32 rb = xcd * 4 + (bi & 3), cb = bi >> 2, prt = rb >> 1;
    const u32 tg = w & 3, hp = w >> 2, r = l & 31, h = l >> 5;
    const u32 wrow = isx ? 3072 : 2752, wrow3 = 3072 + 2752 - wrow;
    const u32 l3 = l >> 3, csw = 16 * ((l & 7) ^ (4 * ((l >> 4) & 1)));
    u32 vx0 = (rt * 128 + 16 * w + l3) * 1024 + 16 * ((l & 7) ^ (l >> 4)), vx1 = (vx0 ^ 64) + 8192;
    u32 vw = (8 * w + (l3 & 3)) * wrow + (isx ? 1024 + ci * 128 : ci * 256) + h * (isx ? 1024 : 128) + csw;
    u32 l16 = l * 16;
    const u32 srow = rt * 128 + (2 * ci + isx) * 8 + w;
    u32 w1k = w * 1024, w2k = w * 2048, k0x = k0 * 128, wh = wrow / 2, w4 = wrow * 4;
    asm volatile("" : "+s"(w1k), "+s"(w2k), "+s"(k0x), "+s"(wh), "+s"(w4));
    PIN(vx0); PIN(vx1); PIN(vw); PIN(l16);
    asm volatile("s_waitcnt lgkmcnt(0)" : "+s"(ka), "+s"(kb));
    u64 epoch;
    asm volatile("s_load_dwordx2 %0, %1, 0" : "=s"(epoch) : "s"((u64)ka[6] | ((u64)ka[7] << 32)));
    const u32x4 srdx = mk_srd(ka[0], ka[1], 2048 * 1024);
    const u32x4 srdw = mk_srd(isx ? ka[2] : ka[4], isx ? ka[3] : ka[5], 512 * wrow);
    const u32x4 srdw3 = mk_srd(isx ? ka[4] : ka[2], isx ? ka[5] : ka[3], 512 * wrow3);
    const u32x4 srdws = mk_srd(ka[6], ka[7], WS_END);
    const u32x4 srd2 = mk_srd(ka[8], ka[9], 1376 * 1024), srdo = mk_srd(ka[10], ka[11], 512 * 1024);
    const rsrc_t rws = __builtin_amdgcn_make_buffer_rsrc((void*)((u64)ka[6] | ((u64)ka[7] << 32)), 0, WS_END, 0x00020000);
    u32 vw3, v2, sto, kx, kw, f32k, f8k, t3[3];
    u32x4 row, st, raw[15], hf[22], gt[8], of[8], xr, br;
    u32 hofs[2], go, oo, outo, colo, flo[2], hso;
    u64 fl;
    const u32 kq = 2 * (tg & 1) + hp;
    auto tail2 = [&](int c) { return (32 + 6 * (c & 1) - 5 * hp + t3[(c >> 1) - 8]) * 32768; };
    auto op = [&](auto K, auto A) {
        constexpr int k = K, a = A;
        if constexpr (k == T) {
            constexpr u32 s = ((a >> 2) & 3) * 16384 + (a & 1) * 1024;
            if constexpr ((a & 3) == 0) kx = dma16w<s, (a >> 2) * 128, 0x380>(w2k, vx0, srdx, k0x);
            else if constexpr ((a & 3) == 1) dma16<s>(w2k, vx1, srdx, kx);
            else if constexpr ((a & 3) == 2) kw = dma16m<L_W + s>(w2k, vw, srdw, kx, wh);
            else dma16a<L_W + s>(w2k, vw, srdw, kw, w4);
        } else if constexpr (k == ROW) ld16(row, l16, srdx, srow * 1024);
        else if constexpr (k == TCH) {
            const u32 tl = seq(w, 0, ka[8], seq(w, 1, ka[10], seq(w, 2, ka[2], ka[4]))), th = seq(w, 0, ka[9], seq(w, 1, ka[11], seq(w, 2, ka[3], ka[5])));
            const u32 rs = seq(w, 2, 3072, seq(w, 3, 2752, 1024)), bs = seq(w, 0, 43 * 128, w < 4 ? 2 * rs : 0);
            dma4(L_ZERO + 256, l3 * rs + (l & 7) * 128, mk_srd(tl, th, (b + 1) * bs), b * bs + seq(w, 3, 1728, 0));
        } else if constexpr (k == SWL) dma4(L_SW + w * 256, l * 4, mk_srd(ka[12], ka[13], 16384), ci * 2048 + w * 256);
        else if constexpr (k == B1L) dma4(L_B1, l * 4, mk_srd(ka[14], ka[15], 2752), isx ? 2048 + ci * 128 : ci * 256);
        else if constexpr (k == SLD) ld16c(st, sto, srdws, 0);
        else if constexpr (k == W3) dma16m<a * 8192>(w1k, vw3, srdw3, wrap<a * 128, 0x380>(k0x), wrow3 / 2);
        else if constexpr (k == CELL && a < 16) dma16w<a * 8192, (((a >> 1) + 4) & 7) * 32768 + (a & 1) * 0x80000, 0xb8000>(w1k, v2, srd2, f32k);
        else if constexpr (k == CELL) dma16<a * 8192>(w1k, v2, srd2, tail2(a));
        else if constexpr (k == ECELL) dma16<a * 8192>(w1k, v2, srd2, isx ? OOB : tail2(a));
        else if constexpr (k == RAW && a < 3) ld16(raw[a], v2, srd2, tail2(19 + a));
        else if constexpr (k == RAW && a < 11) ld16(raw[a], v2, srdo, wrap<(a - 3) * 32768, 0x38000>(f32k));
        else if constexpr (k == RAW) ld16(raw[a], v2, srd2, wrap<((a + 1) >> 1) * 32768 + ((a + 1) & 1) * 0x80000, 0xb8000>(f32k));
        else if constexpr (k == GA) ld16c(gt[a], go, srdws, a * REC);
        else if constexpr (k == HL) {
            if constexpr (a < 16 && !(a & 1)) hso = wrap<(a >> 1) * 8192, 0xe000>(f8k);
            else if constexpr (!(a & 1)) { const u32 u = 3 * kq + t3[(a >> 1) - 8]; hso = u == 11 ? OOB : (32 + u - 8 * kq) * 8192; }
            ld16c(hf[a], hofs[a & 1], srdws, hso);
        } else if constexpr (k == FL) ld8c(fl, flo[a], srdws);
        else if constexpr (k == XR) ld16(xr, outo, srdx, 0);
        else if constexpr (k == BR) ld16(br, colo, mk_srd(kb[0], kb[1], 1024), 0);
        else if constexpr (k == OL) ld16c(of[a], oo, srdws, wrap<a * 8192, 0xe000>(f8k));
    };
    auto ops = [&](auto XX, auto S) {
        constexpr bool X = XX;
        constexpr int s = S;
        rep<0, -1 - nth<X>(s, -1)>([&](auto I) { constexpr Op o = opat<X>(nth<X>(s, I)); op(IC<o.kind>{}, IC<o.arg>{}); });
    };
    constexpr std::true_type XT{}; constexpr std::false_type YT{};
    ops(YT, IC<-3>{});
    ops(YT, IC<-2>{});
    asm volatile("s_barrier");
    ops(YT, IC<-1>{});
    const u32 q = (l & 15) >> 2, p = l & 3, blk = (l >> 4) & 1, ps = ((p & 1) << 1) | (p >> 1), g = l >> 4, tk = l & 15;
    const u32 rot = tg ^ (2 * hp), inner = q * 128 + 64 * (hp ^ (q >> 1)) + 32 * blk + 8 * ps;
    u32 xa[4], wa[4];
#pragma unroll
    for (int i = 0; i < 4; ++i) {
        const u32 s = i ^ rot;
        xa[i] = (32 * tg + r) * 128 + 16 * ((2 * s + h) ^ ((r >> 1) & 7));
        wa[i] = L_W + (4 * s + 2 * h) * 1024 + inner;
        PIN(xa[i]); PIN(wa[i]);
    }
    {
        const u32 c3 = (isx ? 2048 : 0) + ci * 128 + csw;
        vw3 = c3 < wrow3 ? (8 * w + l3) * wrow3 + c3 : OOB;
        v2 = (256 * hp + 8 * tg + l3) * 1024 + cb * 128 + 16 * ((l & 7) ^ (2 * (blk | ((w & 1) << 1))));
        sto = WS_ST + (rt * 128 + 32 * tg + r) * 16;
        PIN(vw3); PIN(v2); PIN(sto);
        const u32 f8 = (cb + 2 * (bi & 3)) & 7;
        f32k = f8 * 32768; f8k = f8 * 8192;
        t3[0] = f8 - 3 * ((f8 * 11) >> 5); t3[1] = t3[0] == 2 ? 0 : t3[0] + 1; t3[2] = 3 - t3[0] - t3[1];
        asm volatile("" : "+s"(f32k), "+s"(f8k), "+s"(t3[0]), "+s"(t3[1]), "+s"(t3[2]));
    }
    const u32 hm = (l >> 2) & 1;
    u32 int_a = 0x3f803f80u & (hm - 1), int_b = 0x3f803f80u & -hm, sidx = (l & 1) ? 0xeeee : 0x4444;
    u32x4 selu;
    { const u32 m = (((l >> 3) ^ h) & 1) - 1; selu = u32x4{int_a & m, int_a & m, int_b & m, int_b & m}; }
    u32 hmask = h - 1, csa = L_CS + (hp * 32 + r) * 16;
    u32 b1a = L_B1 + (hp * 32 + (l & 16) + 4 * ((hm << 1) | ((l >> 3) & 1)) + p) * 2;
    PIN(selu); PIN(sidx); PIN(hmask); PIN(csa); PIN(b1a);
    asm volatile("s_waitcnt lgkmcnt(0)" : "+s"(epoch));
    const u64 tag = epoch + 1;
    auto rowsums = [&]() {
        waitv<after<false>(ROW, 0, ST_AT + 1) - 1>(row);
        u32 ones = 0x3f803f80u;
        asm("" : "+s"(ones));
        float s = 0.f, sq = 0.f;
#pragma unroll
        for (int d = 0; d < 4; ++d) {
            const u32 rd = row[d];
            s = __builtin_amdgcn_fdot2_f32_bf16(__builtin_bit_cast(bf16x2, rd), __builtin_bit_cast(bf16x2, ones), s, false);
            sq = __builtin_amdgcn_fdot2_f32_bf16(__builtin_bit_cast(bf16x2, rd), __builtin_bit_cast(bf16x2, rd), sq, false);
        }
        const auto s32 = __builtin_amdgcn_permlane32_swap(__float_as_uint(s), __float_as_uint(sq), false, false);
        float v = __uint_as_float(s32[0]) + __uint_as_float(s32[1]);
        v = dpp_add<0x140>(dpp_add<0x141>(dpp_add<0x4e>(dpp_add<0xb1>(v))));
        const auto s16 = __builtin_amdgcn_permlane16_swap(__float_as_uint(v), __float_as_uint(v), false, false);
        v = __uint_as_float(s16[0]) + __uint_as_float(s16[1]);
        const auto sf = __builtin_amdgcn_permlane32_swap(__float_as_uint(v), __float_as_uint(v), false, false);
        st16c(u32x4{sf[0], sf[1], (u32)tag, (u32)(tag >> 32)}, rws, l == 0 ? WS_ST + srow * 16 : OOB);
    };
    if (ST_AT < 0) rowsums();
    STAMP(1);

    u32x4 xf[32];
    f32x16 acc0 = {}, acc1 = {};
    float rs1, ga, gb, sdv;
    bf16x8 xfix;
    u32 bias2;
    u32x4 wp0, wp1;
    waitv_barrier<after<false>(T, 3, 0)>();
    STAMP(2);
    {
        bf16x8 wc[3][2], wf0, wf1;
        f32x4 cs = {}, cp0, cp1;
        u32 bias0, bias1;
        float mean, var, rs2;
        auto rdx = [&](int m) { xf[m] = L.r128(xa[m & 3] + ((m >> 2) & 3) * 16384); };
        auto rdw = [&](int m, int j) { wc[m % 3][j] = L.tr(wa[m & 3] + ((m >> 2) & 3) * 16384 + 512 * j, 1024); };
        rdx(0); rdw(0, 0); rdw(0, 1); rdx(1); rdw(1, 0); rdw(1, 1);
        ops(YT, IC<0>{});
        rep<0, 32>([&](auto N) {
            constexpr int n = N, t = n >> 2, i = n & 3;
            if constexpr (i == 3 && t < 7) {
                STAMPX(1, 3 + t);
                if constexpr (t == 5) { waitv_barrier<cmin(after<false>(T, 27, 1 + 2 * n), after<false>(SLD, 0, 1 + 2 * n))>(); PIN(st); }
                else waitv_barrier<after<false>(T, 4 * t + 7, 1 + 2 * n)>();
            }
            if constexpr (n == 30) { STAMPX(1, 10); waitv_barrier<after<false>(ECELL, 18, 61)>(); cp0 = L.r128f(csa); cp1 = L.r128f(csa + 1024); }
            SB();
            if constexpr (n == 28) { cs = SMFMA(asfrag(selu), cat(wc[n % 3][0], wc[n % 3][1]), cs, sidx); SB(); }
            acc0 = MFMA32(wc[n % 3][0], asfrag(xf[n]), acc0);
            PIN(acc0);
            SB();
            prio<3 - ((n + 1) & 3)>();
            if constexpr (i == 3 && t < 7) { rdx(n + 1); rdw(n + 1, 0); rdw(n + 1, 1); }
            else if constexpr (i != 2 && n < 30) { rdx(n + 2); rdw(n + 2, 0); }
            ops(YT, IC<1 + 2 * n>{});
            if constexpr (n == 24) {
                mean = __uint_as_float(st[0]) * (1.f / 512.f);
                var = fmaxf(__uint_as_float(st[1]) * (1.f / 512.f) - mean * mean, 0.f);
                rs1 = __builtin_amdgcn_rsqf(var + 1e-5f);
                PIN(rs1); PIN(var); PIN(mean);
            }
            if constexpr (n == 25) { sdv = __builtin_amdgcn_rcpf(rs2); ga = -2.f * 0.7978845608028654f * LOG2E * rs2; gb = ga * 0.044715f * rs2 * rs2; PIN(sdv); PIN(ga); PIN(gb); }
            if constexpr (n == 26) {
                const u32 hs = pk2(-mean, sdv);
                float lm = -mean - __uint_as_float(hs << 16); PIN(lm);
                const u32 ls = pk2(lm, sdv - __uint_as_float(hs & 0xffff0000u));
                u32x4 xu = {(hs & 0xffffu) | (ls << 16), hs, ls >> 16, 0u};
                PIN(xu);
                xfix = asfrag(xu);
            }
            if constexpr (n == 29) L.w32f(csa + h * 1024 + tg * 4, cs[0] + cs[1]);
            if constexpr (n == 31) wf1 = fix_operand(cp1, bias1, hmask);
            SB();
            acc1 = MFMA32(wc[n % 3][1], asfrag(xf[n]), acc1);
            PIN(acc1);
            SB();
            if constexpr (i == 3 && t < 7) { rdx(n + 2); rdw(n + 2, 0); rdw(n + 2, 1); }
            else if constexpr (i != 2 && n < 30) rdw(n + 2, 1);
            ops(YT, IC<2 + 2 * n>{});
            if constexpr (i == 2 && t < 7) cs = SMFMA(asfrag(selu), cat(wc[n % 3][0], wc[n % 3][1]), cs, sidx);
            if constexpr (2 + 2 * n == ST_AT) rowsums();
            if constexpr (n == 22) {
                const u32 b0 = L.r16(b1a), b1 = L.r16(b1a + 128);
                bias0 = b0 & (isx - 1); bias1 = b1 & (isx - 1); bias2 = b0 & -isx;
                PIN(bias0); PIN(bias1); PIN(bias2);
            }
            if constexpr (n == 23) poll16(st, sto, srdws, tag);
            if constexpr (n == 24) { rs2 = rs1 * __builtin_amdgcn_rsqf(var * rs1 * rs1 + 1e-5f); PIN(rs2); }
            if constexpr (n == 30) wf0 = fix_operand(cp0, bias0, hmask);
            SB();
        });
        acc0 = MFMA32(wf0, xfix, acc0);
        acc1 = MFMA32(wf1, xfix, acc1);
        PIN(acc0); PIN(acc1);
        SB();
    }
    STAMP(3); STAMPX(1, 11);
    auto phi_operand = [&]() {
        const u32x4 wpu = __builtin_bit_cast(u32x4, L.tr(L_SW + (2 * hp + (p & 1)) * 512 + (8 * h + q) * 32 + (2 * blk + (p >> 1)) * 8, 128));
        u32 m0 = hm - 1; PIN(m0);
        wp0 = wpu & m0; wp1 = wpu & ~m0;
        PIN(wp0); PIN(wp1);
    };
    auto logits = [&](const f32x16& a) { return MFMA32(asfrag(wp1), asfrag(pack8(a, 1)), MFMA32(asfrag(wp0), asfrag(pack8(a, 0)), f32x16{})); };
    u32 ba[4];
    auto phase_b_addresses = [&](int part) {
        if (part == 0) {
            hofs[0] = WS_H + ((prt * 172 + 32 * kq + g) * 128 + 64 * (rb & 1) + 16 * tg + tk) * 16; hofs[1] = hofs[0] ^ 256;
            PIN(hofs[0]); PIN(hofs[1]);
        } else if (part == 1) {
            const u32 swz = ((q >> 1) | (blk << 1)) ^ (2 * hp);
            ba[0] = 65536 + kq * 4096 + (8 * g + q) * 128 + 32 * swz + 8 * p;
            PIN(ba[0]);
        } else if (part == 2) {
#pragma unroll
            for (int c = 1; c < 4; ++c) { ba[c] = ba[0] ^ (32 * c); PIN(ba[c]); }
        } else if (part == 3) {
            flo[0] = WS_FG + (prt * 8 + ((3 * kq) >> 1) + (l & 1)) * 128; flo[1] = WS_FO + (prt * 8 + 4 * hp + p) * 128;
            PIN(flo[0]); PIN(flo[1]);
        } else if (part == 4) {
            colo = cb * 128 + 64 * hp + 32 * blk + 16 * h; outo = (rb * 64 + 16 * tg + tk) * 1024 + colo;
            PIN(colo); PIN(outo);
        } else if (part == 5) {
            oo = WS_O + ((prt * 64 + 32 * hp + g) * 128 + 64 * (rb & 1) + 16 * tg + tk) * 16;
            PIN(oo);
        }
    };
    auto pass2 = [&](auto XX, auto step, auto sync) {
        u32 wb[4];
#pragma unroll
        for (int i = 0; i < 4; ++i) { wb[i] = (4 * (i ^ rot) + 2 * h) * 512 + inner; PIN(wb[i]); }
        bf16x8 f[3], c0, c1, wf;
        f32x16 acc = {};
        f32x4 cs = {}, cp;
        sync(IC<0>{});
        f[0] = L.tr(wb[0], 512); f[1] = L.tr(wb[1], 512);
        u32x4 sel2 = {int_a, int_a, int_b, int_b};
        PIN(sel2);
        rep<0, 32>([&](auto N) {
            constexpr int n = N;
            if constexpr (n == MIDB || n == 30) sync(N);
            SB();
            acc = MFMA32(f[n % 3], asfrag(xf[n]), acc);
            PIN(acc);
            SB();
            if constexpr (((n + 2) & 3) == 0) prio<3 - (((n + 2) & 15) >> 2)>();
            if constexpr (n + 2 < 32) f[(n + 2) % 3] = L.tr(wb[(n + 2) & 3] + 8192 * ((n + 2) >> 2), 512);
            if constexpr (n >= 2 && n <= 8 && !(n & 1)) { cs = SMFMA(asfrag(sel2), cat(c0, c1), cs, sidx); PIN(cs); SB(); }
            if constexpr (n <= 6 && !(n & 1)) { c0 = L.tr(wb[0] + 8192 * n, 512); c1 = L.tr(wb[0] + 8192 * (n + 1), 512); }
            if constexpr (n == 11) L.w32f(csa + 2048 + tg * 4, cs[0] + cs[1]);
            if constexpr (n == MIDB) cp = L.r128f(csa + 2048);
            if constexpr (n == MIDB + 2) wf = fix_operand(cp, XX ? bias2 : 0, hmask);
            if constexpr (n == MIDB + 4) { acc = MFMA32(wf, xfix, acc); PIN(acc); }
            ops(XX, IC<200 + n>{});
            step(N);
            SB();
        });
        return acc;
    };
    auto gelu2 = [&](float a, float c) {
        const f32x2 gap = {ga, ga}, gbp = {gb, gb}, sdp = {sdv, sdv}, v = {a, c};
        const f32x2 z = v * __builtin_elementwise_fma(v * v, gbp, gap);
        const f32x2 e = {__builtin_amdgcn_exp2f(z[0]), __builtin_amdgcn_exp2f(z[1])};
        const f32x2 d = __builtin_elementwise_fma(e, sdp, sdp);
        const f32x2 y = v * f32x2{__builtin_amdgcn_rcpf(d[0]), __builtin_amdgcn_rcpf(d[1])};
        return pk2(y[0], y[1]);
    };

    auto phase_b = [&](auto XX) {
        constexpr bool X = XX;
        constexpr int SG = X ? X_FG : Y_FG, SO = X ? X_FO : Y_FO;
        f32x4 am[2][4], as[4];
#pragma unroll
        for (int c = 0; c < 4; ++c) { am[0][c] = f32x4{}; am[1][c] = f32x4{}; as[c] = f32x4{}; }
        u32 wl = w1k + l16, wl2 = wl + 65536, wl3 = wl + L_SCR, bb[4];
        s16x4 f[2][8];
#pragma unroll
        for (int m = 0; m < 8; ++m) f[0][m] = L.trh(ba[m >> 1] + 512 * (m & 1));
        PIN(wl); PIN(wl2); PIN(wl3);
        rep<0, 11>([&](auto S) {
            constexpr int s = S, n = s + 1, at = 400 + 8 * s;
            constexpr int wh = after<X>(HL, 2 * s + 1, at);
            if constexpr (s == 4) {
                waitv_barrier<X ? cmin(wh, after<X>(CELL, 7, at)) : wh>();
                flag<1>(w, tag, srdws, (X ? WS_FG : WS_FO) + (rt * 8 + ci) * 128);
                rep<0, 11>([&](auto J) { PIN(raw[J]); });
                STAMP(9);
            } else if constexpr (X && s == 7) waitv_barrier<cmin(wh, after<X>(CELL, 18, at))>();
            else if constexpr (s == 8) waitv_barrier<wh>();
            if constexpr (s == SG) poll<after<X>(FL, 0, at)>(fl, flo[0], srdws, tag);
            if constexpr (s == SO) poll<after<X>(FL, 1, at)>(fl, flo[1], srdws, tag);
            waitv<wh>(hf[2 * s], hf[2 * s + 1]);
            rep<0, 8>([&](auto M) {
                constexpr int m = M, c = m >> 1;
                SB();
                am[m & 1][c] = MFMA16(Lds::join(f[s & 1][2 * c], f[s & 1][2 * c + 1]), asfrag(hf[2 * s + (m & 1)]), am[m & 1][c]);
                PIN(am[m & 1][c]);
                SB();
                if constexpr (m == 0) prio<3 - (s & 3)>();
                if constexpr (n < 11) {
                    if constexpr (!(m & 1) && (n == 4 || n == 8)) { if constexpr (n == 4) { bb[c] = ba[c] - 65536; PIN(bb[c]); } else { ba[c] += 65536; PIN(ba[c]); } }
                    f[n & 1][m] = L.trh((n < 4 || n == 8 || n == 9 ? ba[c] : bb[c]) + (n == 10 ? 0 : (n & 3) * 16384) + 512 * (m & 1));
                }
                ops(XX, IC<at + m>{});
                constexpr int j = 3 * (s - 4) + (m - 2) / 2;
                if constexpr (s >= 4 && m >= 2 && (m & 1) && j < 11) L.w128(j == 0 ? wl3 : (j < 3 ? wl + (j - 1) * 8192 : wl2 + (j - 3) * 8192), raw[j]);
            });
        });
        SB();
        STAMP(10);
        if constexpr (SO == 11) poll<after<X>(FL, 1, 500)>(fl, flo[1], srdws, tag);
        ops(XX, IC<500>{});
        STAMP(11);
        u32 xo = L_XM + w2k + l16;
        PIN(xo);
#pragma unroll
        for (int k = 1; k < 4; ++k) {
            L.w128f(xo + (k - 1) * 16384, am[k >> 1][2 * (k & 1)]);
            L.w128f(xo + (k - 1) * 16384 + 1024, am[k >> 1][2 * (k & 1) + 1]);
        }
        bf16x8 wf[3][4];
#pragma unroll
        for (int c = 0; c < 4; ++c) { bb[c] += 65536 - (tg & 1) * 8192; PIN(bb[c]); }
        waitv<after<X>(BR, 0, 501)>(xr, br);
        const auto b0 = __builtin_amdgcn_permlane16_swap(br[0], br[2], false, false), b1 = __builtin_amdgcn_permlane16_swap(br[1], br[3], false, false);
        const auto x0 = __builtin_amdgcn_permlane16_swap(xr[0], xr[2], false, false), x1 = __builtin_amdgcn_permlane16_swap(xr[1], xr[3], false, false);
        u32 xres[4] = {x0[0], x1[0], x0[1], x1[1]}, xi[3] = {xo ^ 8192, xo ^ 2048, xo ^ 10240};
        f32x4 mm[2];
#pragma unroll
        for (int j = 0; j < 2; ++j) {
            mm[j] = am[0][j] + f32x4{__uint_as_float(b0[j] << 16), __uint_as_float(b0[j] & 0xffff0000u), __uint_as_float(b1[j] << 16), __uint_as_float(b1[j] & 0xffff0000u)};
            PIN(mm[j]); PIN(xres[2 * j]); PIN(xres[2 * j + 1]);
        }
        PIN(xi[0]); PIN(xi[1]); PIN(xi[2]);
        BARRIER();
        f32x4 gate[2];
        {
            f32x4 pm[2][3];
#pragma unroll
            for (int j = 0; j < 2; ++j)
#pragma unroll
                for (int k = 0; k < 3; ++k) pm[j][k] = L.r128f(xi[k] + k * 16384 + j * 1024);
            SB();
#pragma unroll
            for (int j = 0; j < 2; ++j) {
#pragma unroll
                for (int c = 0; c < 4; ++c) wf[j][c] = L.tr(bb[c] + j * 8192, 512);
                const f32x4 m = (mm[j] + pm[j][0]) + (pm[j][1] + pm[j][2]), z = m * -LOG2E;
                const f32x4 d = f32x4{__builtin_amdgcn_exp2f(z[0]), __builtin_amdgcn_exp2f(z[1]), __builtin_amdgcn_exp2f(z[2]), __builtin_amdgcn_exp2f(z[3])} + 1.f;
                gate[j] = m * f32x4{__builtin_amdgcn_rcpf(d[0]), __builtin_amdgcn_rcpf(d[1]), __builtin_amdgcn_rcpf(d[2]), __builtin_amdgcn_rcpf(d[3])};
                PIN(gate[j]);
                SB();
            }
        }
        u32 xs = L_XS + w2k + l16;
        PIN(xs);
        rep<0, 8>([&](auto J) {
            constexpr int j = J;
            waitv<after<X>(OL, j, 501)>(of[j]);
            rep<0, 4>([&](auto C) {
                constexpr int c = j == 7 ? (C + 2) & 3 : C;
                SB();
                as[c] = MFMA16(wf[j % 3][c], asfrag(of[j]), as[c]);
                PIN(as[c]);
                SB();
                if constexpr (C == 0 && !(j & 1)) prio<3 - (j >> 1)>();
                if constexpr (j < 6) wf[(j + 2) % 3][c] = L.tr(bb[c] + (j + 2) * 8192, 512);
            });
        });
        L.w128f(xs, as[2]);
        L.w128f(xs + 1024, as[3]);
        xs ^= 8192;
        f32x4 y0[2];
#pragma unroll
        for (int j = 0; j < 2; ++j) {
            y0[j] = __builtin_elementwise_fma(as[j], gate[j], f32x4{__uint_as_float(xres[2 * j] << 16), __uint_as_float(xres[2 * j] & 0xffff0000u),
                                                                     __uint_as_float(xres[2 * j + 1] << 16), __uint_as_float(xres[2 * j + 1] & 0xffff0000u)});
            PIN(y0[j]);
        }
        BARRIER();
        STAMP(12);
        {
            u32 y[4];
#pragma unroll
            for (int j = 0; j < 2; ++j) {
                const f32x4 v = __builtin_elementwise_fma(L.r128f(xs + j * 1024), gate[j], y0[j]);
                y[2 * j] = pk2(v[0], v[1]); y[2 * j + 1] = pk2(v[2], v[3]);
            }
            const auto s0 = __builtin_amdgcn_permlane16_swap(y[0], y[2], false, false), s1 = __builtin_amdgcn_permlane16_swap(y[1], y[3], false, false);
            const rsrc_t ro = __builtin_amdgcn_make_buffer_rsrc((void*)((u64)kb[2] | ((u64)kb[3] << 32)), 0, 2048 * 1024, 0x00020000);
            __builtin_amdgcn_raw_buffer_store_b128(u32x4{s0[0], s1[0], s0[1], s1[1]}, ro, outo, 0, 0);
        }
    };

    if (isx) {
        {
            phi_operand();
            u32 va = L_V + (32 * tg + r) * 128 + 16 * ((4 * hp + h) ^ (r & 7));
            u32 pa = L_PHI + ((2 * hp + h) * 128 + 32 * tg + r) * 64 + 16 * ((r >> 1) & 3);
            PIN(va); PIN(pa);
            SB();
            const f32x16 pk = logits(acc0);
            acc1 *= rs1;
            L.w128(va, pack8(acc1, 0));
            L.w128(va ^ 32, pack8(acc1, 1));
            f32x2 ep[8], en[8], sp, sn;
            dual_exp(pk, rs1, ep, en, [&](auto I) {
                if constexpr (I == 0) { sp = ep[0]; sn = en[0]; } else { sp += ep[I]; sn += en[I]; }
                if constexpr (I & 1) { SB(); ops(XT, IC<102 + I / 2>{}); }
            });
            const float rp = __builtin_amdgcn_rcpf(sp[0] + sp[1]), rn = __builtin_amdgcn_rcpf(sn[0] + sn[1]);
#pragma unroll
            for (int c = 0; c < 4; ++c) {
                u32x4 o;
#pragma unroll
                for (int e = 0; e < 4; ++e) {
                    const int i = (c & 2) + (e & 1) + 2 * (e & 2);
                    const f32x2 a = (c & 1) ? en[i] * rn : ep[i] * rp;
                    o[e] = pk2(a[0], a[1]);
                }
                L.w128(pa ^ (16 * c), o);
            }
        }
        {
            u32 a0 = L_PHI + (tg * 128 + 4 * g + q) * 64 + 16 * ((p >> 1) ^ (2 * blk + (q >> 1))) + 8 * (p & 1), a1 = a0 ^ 32;
            u32 b0 = L_V + (4 * g + q) * 128 + 16 * ((2 * tg + (p >> 1)) ^ (4 * blk + q)) + 8 * (p & 1);
            u32 ko = WS_PKV + (ci * 16 + rt) * REC + (hp ? (tk == 0 ? 4096 + tg * 64 + l : OOB) : tg * 1024 + l16);
            PIN(a0); PIN(a1); PIN(b0); PIN(ko);
            waitv_barrier<after<true>(W3, 7, 150)>();
            STAMP(4);
            bf16x8 af[4][2], vf[4];
#pragma unroll
            for (int s = 0; s < 4; ++s) { af[s][0] = L.tr(a0 + s * 2048, 1024); vf[s] = L.tr(b0 + s * 4096, 2048); af[s][1] = L.tr(a1 + s * 2048, 1024); }
            if (hp) {
#pragma unroll
                for (int s = 0; s < 4; ++s) vf[s] = asfrag(u32x4{0x3f803f80u, 0x3f803f80u, 0x3f803f80u, 0x3f803f80u});
            }
            f32x4 k0 = {}, k1 = {};
#pragma unroll
            for (int s = 0; s < 4; ++s) { k0 = MFMA16(af[s][0], vf[s], k0); k1 = MFMA16(af[s][1], vf[s], k1); }
            st16c(u32x4{pk2(k0[0], k0[1]), pk2(k0[2], k0[3]), pk2(k1[0], k1[1]), pk2(k1[2], k1[3])}, rws, ko);
            SB();
        }
        STAMP(5);
        u32 fo, h2;
        const f32x16 acc2 = pass2(XT, [&](auto N) {
            constexpr int n = N;
            if constexpr (n == 9) { fo = WS_FH + (prt * 8 + (l & 7)) * 128; PIN(fo); }
            if constexpr (n == 10) { h2 = (2 * ci + hp < 11) ? WS_H + ((rt * 172 + 128 + 8 * ci + 4 * hp + h) * 128 + 32 * tg + r) * 16 : OOB; PIN(h2); }
            if constexpr (n >= 20 && n < 26) phase_b_addresses(n - 20);
            if constexpr (n == 27) ld8c_w0(fl, w, fo, srdws);
        }, [&](auto N) {
            if constexpr (N == MIDB) {
                waitv_barrier<after<true>(KST, 0, 200 + N)>();
                flag<1>(w, tag, srdws, WS_FP + (ci * 16 + rt) * 128);
                STAMP(6);
            } else if constexpr (N == 30) {
                poll_w0<1>(fl, w, fo, srdws, tag);
                BARRIER();
            }
        });
        STAMP(7);
        u32x4 o[2];
        rep<0, 8>([&](auto I) {
            ops(XT, IC<300 + I>{});
            o[I >> 2][I & 3] = gelu2(acc2[2 * I], acc2[2 * I + 1]);
            if constexpr (I & 1) SB();
        });
        st16c(o[0], rws, h2);
        st16c(o[1], rws, h2 + 4096);
        SB();
        ops(XT, IC<310>{});
        waitv_barrier<after<true>(CELL, 3, 311)>();
        STAMP(8);
        phase_b(XT);
#pragma unroll
        for (int n = 0; n < 32; ++n) asm volatile("" : "=v"(xf[n]));
        asm volatile("" : "=v"(acc0), "=v"(acc1), "=v"(xfix));
    }
    u32 isx2 = isx;
    asm volatile("" : "+s"(isx2));
    if (!isx2) {
        {
            u32 ho = WS_H + ((rt * 172 + 16 * ci + 4 * hp + h) * 128 + 32 * tg + r) * 16;
            PIN(ho);
            if (w == 2) { const u32 v = (l >> 1) == 1 ? 0x3f803f80u : 0u; L.w128(L_ZERO + l16, u32x4{v, v, v, v}); }
            u32x4 o;
            rep<0, 16>([&](auto I) {
                constexpr int i = I;
                ops(YT, IC<100 + i>{});
                o[i & 3] = i < 8 ? gelu2(acc0[2 * (i & 7)], acc0[2 * (i & 7) + 1]) : gelu2(acc1[2 * (i & 7)], acc1[2 * (i & 7) + 1]);
                if constexpr ((i & 3) == 3) st16c(o, rws, ho + (i >> 3) * 16384 + ((i >> 2) & 1) * 4096);
                if constexpr (i & 1) SB();
            });
        }
        STAMP(4);
        u32 fo, kvw, fa, dz0, dz1, os;
        u32x4 one;
        const f32x16 acc2 = pass2(YT, [&](auto N) {
            constexpr int n = N;
            if constexpr (n == 9) { fo = blk ? WS_FH + (prt * 8 + (l & 7)) * 128 : WS_FP + (ci * 16 + tk) * 128; PIN(fo); }
            if constexpr (n == 10) { go = WS_PKV + (ci * 16 + 8 * h) * REC + (32 * w + r) * 16; PIN(go); }
            if constexpr (n == 12) phi_operand();
            if constexpr (n == 13) { kvw = L_KV + (32 * w + 16 * h + tk) * 16 + 8 * blk; PIN(kvw); }
            if constexpr (n == 15) { fa = hm == h ? L_KV + (2 * hp + h) * 1024 + (p + 4 * (r >> 3)) * 16 : L_ZERO; PIN(fa); }
            if constexpr (n == 17) {
                const u32 z = L_ZERO + ((p & 2) ? 32 : 128 + (2 * hp + h) * 256 + 32 * (p & 1));
                dz0 = (q == g && !(p & 1)) ? z : L_ZERO; dz1 = (q == g && (p & 1)) ? z : L_ZERO;
                PIN(dz0); PIN(dz1);
            }
            if constexpr (n == 19) {
                const u64 v = (u64)(((l >> 3) ^ (l >> 4)) & 1 ? 0u : 0x3f80u) << (16 * p);
                one = u32x4{(u32)v & (hm - 1), (u32)(v >> 32) & (hm - 1), (u32)v & -hm, (u32)(v >> 32) & -hm};
                PIN(one);
            }
            if constexpr (n >= 20 && n < 26) phase_b_addresses(n - 20);
            if constexpr (n == 26) { ld8c_w0(fl, w, fo, srdws); os = WS_O + ((rt * 64 + 2 * (4 * ci + 2 * hp + h)) * 128 + 32 * tg + r) * 16; PIN(os); }
        }, [&](auto N) {
            if constexpr (N == 0) waitv_barrier<after<false>(W3, 7, 200)>();
            else if constexpr (N == MIDB) {
                waitv_barrier<after<false>(HST, 3, 200 + N)>();
                flag<1>(w, tag, srdws, WS_FH + (rt * 8 + ci) * 128);
                STAMP(5);
            } else {
                poll_w0<0>(fl, w, fo, srdws, tag);
                BARRIER();
                STAMP(6);
                if (w == 7) {
                    const u32 zo = WS_PKV + (ci * 16 + g) * REC + 4096 + tk * 16;
#pragma unroll
                    for (int m = 0; m < 4; ++m) ld16c(of[m], zo, srdws, 4 * m * REC);
                }
            }
        });
        u32x4 pq[4];
        {
            ops(YT, IC<300>{});
            asm volatile("s_waitcnt vmcnt(%4)" : "+v"(raw[11]), "+v"(raw[12]), "+v"(raw[13]), "+v"(raw[14]) : "n"(after<false>(RAW, 14, 301)) : "memory");
            u32 wl = w1k + l16; PIN(wl);
            L.w128(wl + 4 * 8192, raw[11]); L.w128(wl + 5 * 8192, raw[12]);
            SB();
            f32x16 pp = logits(acc2);
            PIN(pp);
            SB();
            ops(YT, IC<301>{});
            L.w128(wl + 6 * 8192, raw[13]); L.w128(wl + 7 * 8192, raw[14]);
            SB();
            f32x2 ep[8], en[8];
            dual_exp(pp, rs1, ep, en, [&](auto) {});
#pragma unroll
            for (int i = 0; i < 8; ++i) { pq[i >> 2][i & 3] = pk2(ep[i][0], ep[i][1]); pq[2 + (i >> 2)][i & 3] = pk2(en[i][0], en[i][1]); }
#pragma unroll
            for (int i = 0; i < 4; ++i) PIN(pq[i]);
            SB();
        }
        STAMP(7);
        {
            f32x4 sm = {};
            rep<0, 8>([&](auto J) {
                waitv<after<false>(GA, J, 310 + J)>(gt[J]);
                sm = MFMA16(asfrag(one), asfrag(gt[J]), sm);
                PIN(sm);
                SB();
                ops(YT, IC<310 + J>{});
                SB();
            });
            if (w == 7) {
                const u64 v = (u64)0x3f80u << (16 * p);
                const u32x4 all = {(u32)v & (hm - 1), (u32)(v >> 32) & (hm - 1), (u32)v & -hm, (u32)(v >> 32) & -hm};
                f32x4 sz = {};
#pragma unroll
                for (int m = 0; m < 4; ++m) { PIN(of[m]); sz = MFMA16(asfrag(all), asfrag(of[m]), sz); }
                L.w64(L_ZERO + 128 + q * 256 + p * 16 + 8 * blk, u32x2{pk2(sz[0], sz[1]), pk2(sz[2], sz[3])});
            }
            L.w64(kvw, u32x2{pk2(sm[0], sm[1]), pk2(sm[2], sm[3])});
        }
        BARRIER();
        {
            u32x4 zf[4], kf[4];
#pragma unroll
            for (int s = 0; s < 4; ++s) zf[s] = L.r128((s < 2 ? dz0 : dz1) + (s & 1) * 16);
#pragma unroll
            for (int s = 0; s < 4; ++s) kf[s] = L.r128(fa + s * 256);
            SB();
            f32x4 dn = {};
#pragma unroll
            for (int s = 0; s < 4; ++s) dn = MFMA16(asfrag(zf[s]), asfrag(pq[s]), dn);
            PIN(dn);
            SB();
            f32x16 np = MFMA32(asfrag(kf[1]), asfrag(pq[1]), MFMA32(asfrag(kf[0]), asfrag(pq[0]), f32x16{}));
            f32x16 nn = MFMA32(asfrag(kf[2]), asfrag(pq[2]), f32x16{});
            PIN(np); PIN(nn);
            SB();
            const float rd = __builtin_amdgcn_rcpf(fmaf(dn[3], dn[0], dn[2] * fmaf(dn[3], 1e-6f, dn[1])));
            float cp = dn[3] * rd; PIN(cp);
            float cn = dn[2] * rd; PIN(cn);
            SB();
            nn = MFMA32(asfrag(kf[3]), asfrag(pq[3]), nn);
            const f32x16 o = __builtin_elementwise_fma(nn, f32x16{} + cn, np * cp);
            st16c(pack8(o, 0), rws, os);
            st16c(pack8(o, 1), rws, os + 2048);
            SB();
            asm volatile("s_nop 0");
            SB();
        }
        STAMP(8);
        phase_b(YT);
    }
    if (b == 0 && w == 0) {
        poll<0>(~tag, WS_FH + l * 128, srdws, tag);
        poll<0>(~tag, WS_FH + (64 + l) * 128, srdws, tag);
        flag<0>(w, tag, srdws, 0);
    }
    asm volatile(".Lpf_end:");
    asm volatile("s_waitcnt vmcnt(0)" ::: "memory");
    STAMP(13);
    if (threadIdx.x == 64 * PROFW) {
        u64* sp = (u64*)((char*)ws_ + WS_PROF) + ((tag & 15) * 256 + b) * 16;
        stamps[14] = !isx;
#pragma unroll
        for (int i = 0; i < 16; ++i) sp[i] = stamps[i];
    }
}

extern "C" void kernel_launch(void* const* d_in, const int* in_sizes, int n_in, void* d_out, int out_size, void* d_ws, size_t ws_size,
                              hipStream_t stream) {
    hedgehog_mixer_kernel<<<256, 512, 0, stream>>>(d_in[0], d_in[1], d_in[5], d_ws, d_in[7], d_in[3], d_in[2], d_in[6], d_in[8], d_out);
}
```

```cpp
#define PROF 1
#define SSET 2
#include <hip/hip_runtime.h>
#include <hip/hip_bf16.h>
#include <type_traits>

typedef unsigned u32;
typedef unsigned long long u64;
typedef __bf16 bf16x2 __attribute__((ext_vector_type(2)));
typedef __bf16 bf16x8 __attribute__((ext_vector_type(8)));
typedef __bf16 bf16x16 __attribute__((ext_vector_type(16)));
typedef short s16x4 __attribute__((ext_vector_type(4)));
typedef float f32x2 __attribute__((ext_vector_type(2)));
typedef float f32x4 __attribute__((ext_vector_type(4)));
typedef float f32x16 __attribute__((ext_vector_type(16)));
typedef u32 u32x2 __attribute__((ext_vector_type(2)));
typedef u32 u32x4 __attribute__((ext_vector_type(4)));
typedef u32 u32x16 __attribute__((ext_vector_type(16)));
#define LAS __attribute__((address_space(3)))
#define DI __device__ __forceinline__
#define SB() __builtin_amdgcn_sched_barrier(0)
template <int N> using IC = std::integral_constant<int, N>;
template <int A, int B, class F> DI void rep(F f) { if constexpr (A < B) { f(IC<A>{}); rep<A + 1, B>(f); } }

#define PROFW 0
#define ROTMODE 8
#define RPOS 2
#define PRIO 1
#define MIDB 14

constexpr u32 WS_FP = 4096;
constexpr u32 WS_FH = WS_FP + 128 * 128;
constexpr u32 WS_FG = WS_FH + 128 * 128;
constexpr u32 WS_FO = WS_FG + 128 * 128;
constexpr u32 WS_ST = WS_FO + 128 * 128;
constexpr u32 REC = 4352;
constexpr u32 WS_PKV = WS_ST + 2048 * 16;
constexpr u32 WS_O = WS_PKV + 128 * REC;
constexpr u32 WS_H = WS_O + 16 * 64 * 2048;
constexpr u32 WS_END = WS_H + 16 * 172 * 2048;
constexpr u32 WS_PROF = 16u << 20;
constexpr u32 OOB = 0x7ff00000u;
static_assert(WS_O % 2048 == 0, "");

constexpr u32 L_W = 65536;
constexpr u32 L_PHI = 13 * 8192;
constexpr u32 L_V = 17 * 8192;
constexpr u32 L_SCR = 19 * 8192;
constexpr u32 L_B1 = L_SCR;
constexpr u32 L_CS = L_SCR + 2048;
constexpr u32 L_SW = L_SCR + 5120;
constexpr u32 L_ZERO = L_SCR + 7168;
constexpr u32 L_KV = L_SCR;
constexpr u32 L_XM = 2 * 8192;
constexpr u32 L_XS = 16 * 8192;

constexpr float LOG2E = 1.4426950408889634f;

enum { T, ROW, TCH, SWL, B1L, SST, SLD, W3, CELL, ECELL, RAW, HST, KST, GA, HL, OST, FL, XR, BR, OL };
struct Op { int at, kind, arg; };
#define TILE(t, a, b, c, d) {a, T, 4 * t}, {b, T, 4 * t + 1}, {c, T, 4 * t + 2}, {d, T, 4 * t + 3}
constexpr int ST_AT = RPOS == 0 ? -1 : (RPOS == 1 ? 6 : 14);
#define COMMON_OPS \
    {RPOS == 0 ? -3 : (RPOS == 1 ? -2 : -1), ROW, 0}, {ST_AT, SST, 0}, \
    TILE(0, -2, -2, -2, -2), TILE(1, -1, -1, -1, -1), TILE(2, 0, 2, 4, 5), TILE(3, 8, 9, 10, 11), TILE(4, 12, 13, 13, 14), \
    {11, TCH, 0}, {11, SWL, 0}, {11, B1L, 0}, \
    TILE(5, 16, 18, 20, 21), TILE(6, 24, 26, 28, 29), {30, ECELL, 16}, {31, SLD, 0}, TILE(7, 32, 34, 36, 37), {38, ECELL, 17}, \
    {40, W3, 0}, {42, W3, 1}, {44, CELL, 8}, {45, CELL, 9}, {46, ECELL, 18}, {48, W3, 2}, {50, W3, 3}, {52, CELL, 10}, {53, CELL, 11}, \
    {56, W3, 4}, {58, W3, 5}, {60, CELL, 12}, {61, W3, 6}, {63, W3, 7}
#define B1_H(s, j) {400 + 8 * (s) + 1, HL, j}, {400 + 8 * (s) + 5, HL, (j) + 1}
constexpr Op YOPS[] = { COMMON_OPS,
    {100, CELL, 13}, {101, CELL, 14}, {102, CELL, 15}, {103, HST, 0}, {105, RAW, 0}, {106, RAW, 1}, {107, HST, 1}, {109, RAW, 2}, {110, RAW, 3},
    {111, HST, 2}, {113, RAW, 4}, {115, HST, 3},
    {201, RAW, 5}, {203, RAW, 6}, {205, RAW, 7}, {207, RAW, 8}, {209, RAW, 9}, {211, RAW, 10}, {213, RAW, 11},
    {215, RAW, 12}, {217, RAW, 13}, {219, RAW, 14}, {220, CELL, 0}, {222, CELL, 1}, {224, CELL, 2}, {226, CELL, 3},
    {230, GA, 0}, {230, GA, 1}, {231, GA, 2}, {231, GA, 3}, {300, GA, 4}, {300, GA, 5}, {301, GA, 6}, {301, GA, 7},
    {310, HL, 0}, {311, HL, 1}, {312, HL, 2}, {313, HL, 3}, {314, HL, 4}, {315, HL, 5}, {316, HL, 6}, {317, HL, 7},
    {330, OST, 0}, {330, OST, 1},
    B1_H(0, 8), B1_H(1, 10), {415, FL, 0}, B1_H(2, 12), B1_H(3, 14), B1_H(4, 16), B1_H(5, 18), B1_H(6, 20), {455, FL, 1},
    {457, XR, 0}, {461, BR, 0},
    {472, OL, 0}, {474, OL, 1}, {476, OL, 2}, {478, OL, 3}, {480, OL, 4}, {482, OL, 5}, {484, OL, 6}, {486, OL, 7} };
constexpr int Y_FG = 4, Y_FO = 9;
constexpr Op XOPS[] = { COMMON_OPS,
    {102, RAW, 0}, {103, RAW, 1}, {104, RAW, 2}, {105, RAW, 3}, {150, KST, 0},
    {203, RAW, 4}, {205, RAW, 5}, {207, RAW, 6}, {209, RAW, 7}, {211, RAW, 8}, {213, RAW, 9},
    {215, CELL, 13}, {217, CELL, 14}, {219, CELL, 15}, {221, CELL, 0}, {223, CELL, 1}, {225, CELL, 2}, {227, CELL, 3}, {229, RAW, 10},
    {230, HL, 0}, {231, HL, 1}, {300, HL, 2}, {301, HL, 3}, {302, HL, 4}, {303, HL, 5}, {304, HL, 6}, {305, HL, 7}, {307, HST, 0}, {307, HST, 1},
    {310, CELL, 4}, {310, CELL, 5}, {403, CELL, 6}, {407, CELL, 7},
    B1_H(0, 8), B1_H(1, 10), B1_H(2, 12), B1_H(3, 14), {433, CELL, 16}, {437, CELL, 17}, {441, CELL, 18}, {445, XR, 0}, {447, BR, 0}, {451, FL, 0},
    {456, HL, 16}, {457, HL, 17}, {458, HL, 18}, {459, HL, 19}, {461, HL, 20}, {463, HL, 21}, {479, FL, 1},
    {500, OL, 0}, {500, OL, 1}, {500, OL, 2}, {500, OL, 3}, {500, OL, 4}, {500, OL, 5}, {500, OL, 6}, {500, OL, 7} };
constexpr int X_FG = 7, X_FO = 11;
template <bool X> constexpr int nops() { return X ? sizeof(XOPS) / sizeof(Op) : sizeof(YOPS) / sizeof(Op); }
template <bool X> constexpr Op opat(int i) { return X ? XOPS[i] : YOPS[i]; }
constexpr bool is_store(int k) { return k == SST || k == HST || k == KST || k == OST; }
template <bool X> constexpr long long okey(int i) { return (opat<X>(i).at * 4LL + (is_store(opat<X>(i).kind) ? 2 : (opat<X>(i).kind == ROW ? 1 : 0))) * 1024 + i; }
template <bool X> constexpr int after(int kind, int arg, int slot) {
    int me = -1, c = 0;
    for (int i = 0; i < nops<X>(); ++i) if (opat<X>(i).kind == kind && opat<X>(i).arg == arg) me = i;
    if (me < 0 || opat<X>(me).at >= slot) return -1000;
    for (int i = 0; i < nops<X>(); ++i) if (okey<X>(i) > okey<X>(me) && opat<X>(i).at < slot) ++c;
    return c;
}
constexpr int cmin(int a, int b) { return a < b ? a : b; }
template <bool X> constexpr int nth(int slot, int j) {
    int c = 0;
    for (int p = 0; p < 2; ++p)
        for (int i = 0; i < nops<X>(); ++i)
            if (opat<X>(i).at == slot && !is_store(opat<X>(i).kind) && (opat<X>(i).kind == ROW) == p && c++ == j) return i;
    return -1 - c;
}

DI u32x4 mk_srd(u32 lo, u32 hi, u32 bytes) { return u32x4{lo, hi & 0xffffu, bytes, 0x00020000u}; }
DI u32 seq(u32 x, u32 c, u32 a, u32 b) {
    u32 r;
    asm("s_cmp_eq_u32 %1, %2\n\ts_cselect_b32 %0, %3, %4" : "=s"(r) : "s"(x), "n"(c), "s"(a), "s"(b) : "scc");
    return r;
}
template <u32 A, u32 M> DI u32 wrap(u32 f) {
    u32 r;
    asm volatile("s_add_u32 %0, %1, %2\n\ts_and_b32 %0, %0, %3" : "=s"(r) : "s"(f), "n"(A), "n"(M) : "scc");
    return r;
}
template <u32 I> DI void dma16(u32 lb, u32 voff, u32x4 srd, u32 soff) {
    asm volatile("s_add_u32 m0, %0, %4\n\ts_nop 0\n\tbuffer_load_dwordx4 %1, %2, %3 offen lds"
                 :: "s"(lb), "v"(voff), "s"(srd), "s"(soff), "n"(I) : "memory", "scc");
}
template <u32 I> DI void dma16a(u32 lb, u32 voff, u32x4 srd, u32 sa, u32 sb) {
    u32 t;
    asm volatile("s_add_u32 m0, %1, %6\n\ts_add_u32 %0, %4, %5\n\tbuffer_load_dwordx4 %2, %3, %0 offen lds"
                 : "=&s"(t) : "s"(lb), "v"(voff), "s"(srd), "s"(sa), "s"(sb), "n"(I) : "memory", "scc");
}
template <u32 I> DI u32 dma16m(u32 lb, u32 voff, u32x4 srd, u32 sa, u32 sb) {
    u32 t;
    asm volatile("s_add_u32 m0, %1, %6\n\ts_mul_i32 %0, %4, %5\n\tbuffer_load_dwordx4 %2, %3, %0 offen lds"
                 : "=&s"(t) : "s"(lb), "v"(voff), "s"(srd), "s"(sa), "s"(sb), "n"(I) : "memory", "scc");
    return t;
}
template <u32 I, u32 A, u32 M> DI u32 dma16w(u32 lb, u32 voff, u32x4 srd, u32 f) {
    u32 t;
    asm volatile("s_add_u32 m0, %1, %5\n\ts_add_u32 %0, %4, %6\n\ts_and_b32 %0, %0, %7\n\tbuffer_load_dwordx4 %2, %3, %0 offen lds"
                 : "=&s"(t) : "s"(lb), "v"(voff), "s"(srd), "s"(f), "n"(I), "n"(A), "n"(M) : "memory", "scc");
    return t;
}
DI void dma4(u32 lds, u32 voff, u32x4 srd, u32 soff) {
    asm volatile("s_mov_b32 m0, %0\n\ts_nop 0\n\tbuffer_load_dword %1, %2, %3 offen lds" :: "s"(lds), "v"(voff), "s"(srd), "s"(soff) : "memory");
}
DI void ld16(u32x4& d, u32 voff, u32x4 srd, u32 soff) {
    asm volatile("buffer_load_dwordx4 %0, %1, %2, %3 offen" : "=v"(d) : "v"(voff), "s"(srd), "s"(soff) : "memory");
}
DI void ld16c(u32x4& d, u32 voff, u32x4 srd, u32 soff) {
    asm volatile("buffer_load_dwordx4 %0, %1, %2, %3 offen sc1" : "=v"(d) : "v"(voff), "s"(srd), "s"(soff) : "memory");
}
DI void ld8c(u64& d, u32 voff, u32x4 srd) {
    asm volatile("buffer_load_dwordx2 %0, %1, %2, 0 offen sc1" : "=v"(d) : "v"(voff), "s"(srd) : "memory");
}
typedef __amdgpu_buffer_rsrc_t rsrc_t;
DI void st16c(u32x4 v, rsrc_t r, u32 voff) { __builtin_amdgcn_raw_buffer_store_b128(v, r, voff, 0, 16); }
#define BARRIER() asm volatile("s_waitcnt lgkmcnt(0)\n\ts_barrier" ::: "memory")
template <int N> DI void waitv_barrier() { static_assert(N >= 0 && N < 64, ""); asm volatile("s_waitcnt vmcnt(%0) lgkmcnt(0)\n\ts_barrier" :: "n"(N) : "memory"); }
template <int N, class V> DI void waitv(V& r) { static_assert(N >= 0 && N < 64, ""); asm volatile("s_waitcnt vmcnt(%1)" : "+v"(r) : "n"(N) : "memory"); }
template <class V> DI void PIN(V& r) { asm volatile("" : "+v"(r)); }
template <int N, class V> DI void waitv(V& r, V& s) { static_assert(N >= 0 && N < 64, ""); asm volatile("s_waitcnt vmcnt(%2)" : "+v"(r), "+v"(s) : "n"(N) : "memory"); }
template <int P> DI void prio() { if (PRIO) asm volatile("s_setprio %0" :: "n"(P)); }
template <int K> DI void flag(u32 w, u64 tag, u32x4 srd, u32 soff) {
    u64 t;
    asm volatile("s_cmp_lg_u32 %1, %5\n\ts_cbranch_scc1 .Lf%=\n\ts_mov_b64 exec, 1\n\tv_mov_b64 %0, %2\n\t"
                 "buffer_store_dwordx2 %0, off, %3, %4 sc1\n\ts_mov_b64 exec, -1\n.Lf%=:"
                 : "=&v"(t) : "s"(w), "s"(tag), "s"(srd), "s"(soff), "n"(K) : "memory", "scc");
}
#define POLL_LOOP(o) "v_cmp_ne_u64 vcc, %[tag], %[f]\n\ts_cbranch_vccz .Ld%=\n\ts_mov_b32 m0, 0x40000\n.Lp%=:\n\ts_sleep 1\n\t" \
    "buffer_load_dwordx2 %[f], %[off], %[srd], 0 offen offset:" #o " sc1\n\ts_sub_u32 m0, m0, 1\n\ts_waitcnt vmcnt(0)\n\ts_cbranch_scc1 .Le%=\n\t" \
    "v_cmp_ne_u64 vcc, %[tag], %[f]\n\ts_cbranch_vccnz .Lp%=\n.Le%=:\n\t"
template <int N> DI void poll(u64 f, u32 off, u32x4 srd, u64 tag) {
    static_assert(N >= 0 && N < 64, "");
    asm volatile("s_waitcnt vmcnt(%[n])\n\t" POLL_LOOP(0) "\n.Ld%=:" : [f] "+v"(f) : [off] "v"(off), [srd] "s"(srd), [tag] "s"(tag), [n] "n"(N) : "memory", "scc", "vcc");
}
DI void ld8c_w0(u64& f, u32 w, u32 off, u32x4 srd) {
    asm volatile("s_cmp_lg_u32 %1, 0\n\ts_cbranch_scc1 .Ld%=\n\tbuffer_load_dwordx2 %0, %2, %3, 0 offen sc1\n.Ld%=:"
                 : "=v"(f) : "s"(w), "v"(off), "s"(srd) : "memory", "scc");
}
template <int N> DI void poll_w0(u64 f, u32 w, u32 off, u32x4 srd, u64 tag) {
    asm volatile("s_cmp_lg_u32 %[w], 0\n\ts_cbranch_scc1 .Ld%=\n\ts_waitcnt vmcnt(%[n])\n\t" POLL_LOOP(0) "\n.Ld%=:"
                 : [f] "+v"(f) : [w] "s"(w), [off] "v"(off), [srd] "s"(srd), [tag] "s"(tag), [n] "n"(N) : "memory", "scc", "vcc");
}
DI void poll16(u32x4& st, u32 off, u32x4 srd, u64 tag) {
    u64 f = (u64)st[2] | ((u64)st[3] << 32);
    asm volatile(POLL_LOOP(8) "buffer_load_dwordx4 %[st], %[off], %[srd], 0 offen sc1\n\ts_waitcnt vmcnt(0)\n.Ld%=:"
                 : [f] "+v"(f), [st] "+v"(st) : [off] "v"(off), [srd] "s"(srd), [tag] "s"(tag) : "memory", "scc", "vcc");
}

DI u32 pk2(float a, float b) {
    f32x2 f = {a, b};
    return __builtin_bit_cast(u32, __builtin_convertvector(f, bf16x2));
}
DI bf16x8 asfrag(u32x4 v) { return __builtin_bit_cast(bf16x8, v); }
DI bf16x16 cat(bf16x8 a, bf16x8 b) { return __builtin_shufflevector(a, b, 0, 1, 2, 3, 4, 5, 6, 7, 8, 9, 10, 11, 12, 13, 14, 15); }
#define MFMA32(a, b, c) __builtin_amdgcn_mfma_f32_32x32x16_bf16((a), (b), (c), 0, 0, 0)
#define MFMA16(a, b, c) __builtin_amdgcn_mfma_f32_16x16x32_bf16((a), (b), (c), 0, 0, 0)
#define SMFMA(a, b, c, i) __builtin_amdgcn_smfmac_f32_16x16x64_bf16((a), (b), (c), (i), 0, 0)

struct Lds {
    LAS char* p;
    DI u32x4 r128(u32 a) const { return *(LAS u32x4*)(p + a); }
    DI f32x4 r128f(u32 a) const { return *(LAS f32x4*)(p + a); }
    DI void w128(u32 a, u32x4 v) const { *(LAS u32x4*)(p + a) = v; }
    DI void w128f(u32 a, f32x4 v) const { *(LAS f32x4*)(p + a) = v; }
    DI void w64(u32 a, u32x2 v) const { *(LAS u32x2*)(p + a) = v; }
    DI void w32f(u32 a, float v) const { *(LAS float*)(p + a) = v; }
    DI u32 r16(u32 a) const { return *(LAS unsigned short*)(p + a); }
    DI s16x4 trh(u32 a) const { return __builtin_amdgcn_ds_read_tr16_b64_v4i16((LAS s16x4*)(p + a)); }
    static DI bf16x8 join(s16x4 lo, s16x4 hi) { return __builtin_bit_cast(bf16x8, __builtin_shufflevector(lo, hi, 0, 1, 2, 3, 4, 5, 6, 7)); }
    DI bf16x8 tr(u32 a, u32 d) const { return join(trh(a), trh(a + d)); }
};

template <class F> DI void dual_exp(const f32x16& p, float sc, f32x2 (&ep)[8], f32x2 (&en)[8], F hook) {
    float mx = p[0], mn = p[0];
#pragma unroll
    for (int i = 1; i < 16; ++i) { mx = __builtin_elementwise_maximum(mx, p[i]); mn = __builtin_elementwise_minimum(mn, p[i]); }
    const float c = sc * LOG2E;
    const f32x2 cp = {c, c}, cn = {-c, -c};
    const float bx = -mx * c, bn = mn * c;
    const f32x2 bxp = {bx, bx}, bnp = {bn, bn};
    rep<0, 8>([&](auto I) {
        constexpr int i = I;
        const f32x2 v = {p[2 * i], p[2 * i + 1]};
        const f32x2 a = __builtin_elementwise_fma(v, cp, bxp), b = __builtin_elementwise_fma(v, cn, bnp);
        ep[i] = f32x2{__builtin_amdgcn_exp2f(a[0]), __builtin_amdgcn_exp2f(a[1])};
        en[i] = f32x2{__builtin_amdgcn_exp2f(b[0]), __builtin_amdgcn_exp2f(b[1])};
        hook(I);
    });
}
DI u32x4 pack8(const f32x16& a, int s) {
    return u32x4{pk2(a[8 * s], a[8 * s + 1]), pk2(a[8 * s + 2], a[8 * s + 3]), pk2(a[8 * s + 4], a[8 * s + 5]), pk2(a[8 * s + 6], a[8 * s + 7])};
}
DI bf16x8 fix_operand(f32x4 cp, u32 bias, u32 hmask) {
    float a = cp[0] + cp[1]; PIN(a);
    a += cp[2]; PIN(a);
    const float c0 = a + cp[3];
    const u32 chh = pk2(c0, c0);
    const u32 cl = pk2(c0 - __uint_as_float(chh << 16), 0.f);
    u32x4 wf = {chh & hmask, (cl | (bias << 16)) & hmask, bias & hmask, 0u};
    PIN(wf);
    return asfrag(wf);
}
template <int C> DI float dpp_add(float v) {
    return v + __int_as_float(__builtin_amdgcn_update_dpp(0, __float_as_int(v), C, 15, 15, true));
}

#define STAMPOP "s_memrealtime"
DI void stamp(u64& t) { asm volatile(STAMPOP " %0\n\ts_waitcnt lgkmcnt(0)" : "=s"(t)); }
#define STAMPX(set, i) do { if (set == SSET || i == 0 || i == 13) { SB(); stamp(stamps[i]); SB(); } } while (0)
#define STAMP(i) STAMPX(0, i)

__global__ void __launch_bounds__(512, 2)
hedgehog_mixer_kernel(const void* x_, const void* wqkv_, const void* w1_, void* ws_, const void* w2_, const void* wo_, const void* wphi_,
                      const void* b1_, const void* b2_, void* out_) {
    __shared__ __attribute__((aligned(16))) char smem[163840];
    const Lds L{(LAS char*)smem};
    u64 stamps[16];
#pragma unroll
    for (int i = 0; i < 16; ++i) stamps[i] = 0;
    STAMP(0);
    u32x16 ka; u32x4 kb;
    {
        const u64 kp = (u64)__builtin_amdgcn_kernarg_segment_ptr();
        asm volatile("s_load_dwordx16 %0, %2, 0\n\ts_load_dwordx4 %1, %2, 64" : "=&s"(ka), "=&s"(kb) : "s"(kp));
    }
    const u32 b = blockIdx.x, xcd = b & 7, bi = b >> 3;
    const u32 l = threadIdx.x & 63, w = __builtin_amdgcn_readfirstlane(threadIdx.x >> 6);
    {
        u64 pc; u32 len;
        asm volatile("s_getpc_b64 %0\n.Lpf_here:\n\ts_mov_b32 %1, .Lpf_end-.Lpf_here" : "=s"(pc), "=s"(len));
        const u32x4 sc = mk_srd((u32)pc, (u32)(pc >> 32), len);
        asm volatile("s_mov_b32 m0, %0\n\ts_nop 3\n\tbuffer_load_dword off, %1, %2 lds" :: "n"(L_ZERO), "s"(sc), "s"((w * 32 + bi) * 128) : "memory");
    }
    const u32 rt = (xcd & 1) * 8 + (bi & 7), isx = (bi >> 3) & 1, ci = (xcd >> 1) * 2 + (bi >> 4);
    const u32 k0 = rt & 7;
    const u32 rb = xcd * 4 + (bi & 3), cb = bi >> 2, prt = rb >> 1;
    const u32 tg = w & 3, hp = w >> 2, r = l & 31, h = l >> 5;
    const u32 wrow = isx ? 3072 : 2752, wrow3 = 3072 + 2752 - wrow;
    const u32 l3 = l >> 3, csw = 16 * ((l & 7) ^ (4 * ((l >> 4) & 1)));
    u32 vx0 = (rt * 128 + 16 * w + l3) * 1024 + 16 * ((l & 7) ^ (l >> 4)), vx1 = (vx0 ^ 64) + 8192;
    u32 vw = (8 * w + (l3 & 3)) * wrow + (isx ? 1024 + ci * 128 : ci * 256) + h * (isx ? 1024 : 128) + csw;
    u32 l16 = l * 16;
    const u32 srow = rt * 128 + (2 * ci + isx) * 8 + w;
    u32 w1k = w * 1024, w2k = w * 2048, k0x = k0 * 128, wh = wrow / 2, w4 = wrow * 4;
    asm volatile("" : "+s"(w1k), "+s"(w2k), "+s"(k0x), "+s"(wh), "+s"(w4));
    PIN(vx0); PIN(vx1); PIN(vw); PIN(l16);
    asm volatile("s_waitcnt lgkmcnt(0)" : "+s"(ka), "+s"(kb));
    STAMPX(2, 1);
    u64 epoch;
    asm volatile("s_load_dwordx2 %0, %1, 0" : "=s"(epoch) : "s"((u64)ka[6] | ((u64)ka[7] << 32)));
    const u32x4 srdx = mk_srd(ka[0], ka[1], 2048 * 1024);
    const u32x4 srdw = mk_srd(isx ? ka[2] : ka[4], isx ? ka[3] : ka[5], 512 * wrow);
    const u32x4 srdw3 = mk_srd(isx ? ka[4] : ka[2], isx ? ka[5] : ka[3], 512 * wrow3);
    const u32x4 srdws = mk_srd(ka[6], ka[7], WS_END);
    const u32x4 srd2 = mk_srd(ka[8], ka[9], 1376 * 1024), srdo = mk_srd(ka[10], ka[11], 512 * 1024);
    const rsrc_t rws = __builtin_amdgcn_make_buffer_rsrc((void*)((u64)ka[6] | ((u64)ka[7] << 32)), 0, WS_END, 0x00020000);
    u32 vw3, v2, sto, kx, kw, f32k, f8k, t3[3];
    u32x4 row, st, raw[15], hf[22], gt[8], of[8], xr, br;
    u32 hofs[2], go, oo, outo, colo, flo[2], hso;
    u64 fl;
    const u32 kq = 2 * (tg & 1) + hp;
    auto tail2 = [&](int c) { return (32 + 6 * (c & 1) - 5 * hp + t3[(c >> 1) - 8]) * 32768; };
    auto op = [&](auto K, auto A) {
        constexpr int k = K, a = A;
        if constexpr (k == T) {
            constexpr u32 s = ((a >> 2) & 3) * 16384 + (a & 1) * 1024;
            if constexpr ((a & 3) == 0) kx = dma16w<s, (a >> 2) * 128, 0x380>(w2k, vx0, srdx, k0x);
            else if constexpr ((a & 3) == 1) dma16<s>(w2k, vx1, srdx, kx);
            else if constexpr ((a & 3) == 2) kw = dma16m<L_W + s>(w2k, vw, srdw, kx, wh);
            else dma16a<L_W + s>(w2k, vw, srdw, kw, w4);
        } else if constexpr (k == ROW) ld16(row, l16, srdx, srow * 1024);
        else if constexpr (k == TCH) {
            const u32 tl = seq(w, 0, ka[8], seq(w, 1, ka[10], seq(w, 2, ka[2], ka[4]))), th = seq(w, 0, ka[9], seq(w, 1, ka[11], seq(w, 2, ka[3], ka[5])));
            const u32 rs = seq(w, 2, 3072, seq(w, 3, 2752, 1024)), bs = seq(w, 0, 43 * 128, w < 4 ? 2 * rs : 0);
            dma4(L_ZERO + 256, l3 * rs + (l & 7) * 128, mk_srd(tl, th, (b + 1) * bs), b * bs + seq(w, 3, 1728, 0));
        } else if constexpr (k == SWL) dma4(L_SW + w * 256, l * 4, mk_srd(ka[12], ka[13], 16384), ci * 2048 + w * 256);
        else if constexpr (k == B1L) dma4(L_B1, l * 4, mk_srd(ka[14], ka[15], 2752), isx ? 2048 + ci * 128 : ci * 256);
        else if constexpr (k == SLD) ld16c(st, sto, srdws, 0);
        else if constexpr (k == W3) dma16m<a * 8192>(w1k, vw3, srdw3, wrap<a * 128, 0x380>(k0x), wrow3 / 2);
        else if constexpr (k == CELL && a < 16) dma16w<a * 8192, (((a >> 1) + 4) & 7) * 32768 + (a & 1) * 0x80000, 0xb8000>(w1k, v2, srd2, f32k);
        else if constexpr (k == CELL) dma16<a * 8192>(w1k, v2, srd2, tail2(a));
        else if constexpr (k == ECELL) dma16<a * 8192>(w1k, v2, srd2, isx ? OOB : tail2(a));
        else if constexpr (k == RAW && a < 3) ld16(raw[a], v2, srd2, tail2(19 + a));
        else if constexpr (k == RAW && a < 11) ld16(raw[a], v2, srdo, wrap<(a - 3) * 32768, 0x38000>(f32k));
        else if constexpr (k == RAW) ld16(raw[a], v2, srd2, wrap<((a + 1) >> 1) * 32768 + ((a + 1) & 1) * 0x80000, 0xb8000>(f32k));
        else if constexpr (k == GA) ld16c(gt[a], go, srdws, a * REC);
        else if constexpr (k == HL) {
            if constexpr (a < 16 && !(a & 1)) hso = wrap<(a >> 1) * 8192, 0xe000>(f8k);
            else if constexpr (!(a & 1)) { const u32 u = 3 * kq + t3[(a >> 1) - 8]; hso = u == 11 ? OOB : (32 + u - 8 * kq) * 8192; }
            ld16c(hf[a], hofs[a & 1], srdws, hso);
        } else if constexpr (k == FL) ld8c(fl, flo[a], srdws);
        else if constexpr (k == XR) ld16(xr, outo, srdx, 0);
        else if constexpr (k == BR) ld16(br, colo, mk_srd(kb[0], kb[1], 1024), 0);
        else if constexpr (k == OL) ld16c(of[a], oo, srdws, wrap<a * 8192, 0xe000>(f8k));
    };
    auto ops = [&](auto XX, auto S) {
        constexpr bool X = XX;
        constexpr int s = S;
        rep<0, -1 - nth<X>(s, -1)>([&](auto I) { constexpr Op o = opat<X>(nth<X>(s, I)); op(IC<o.kind>{}, IC<o.arg>{}); });
    };
    constexpr std::true_type XT{}; constexpr std::false_type YT{};
    ops(YT, IC<-3>{});
    ops(YT, IC<-2>{});
    STAMPX(2, 2);
    asm volatile("s_barrier");
    STAMPX(2, 3);
    ops(YT, IC<-1>{});
    STAMPX(2, 4);
    const u32 q = (l & 15) >> 2, p = l & 3, blk = (l >> 4) & 1, ps = ((p & 1) << 1) | (p >> 1), g = l >> 4, tk = l & 15;
    const u32 rot = tg ^ (2 * hp), inner = q * 128 + 64 * (hp ^ (q >> 1)) + 32 * blk + 8 * ps;
    u32 xa[4], wa[4];
#pragma unroll
    for (int i = 0; i < 4; ++i) {
        const u32 s = i ^ rot;
        xa[i] = (32 * tg + r) * 128 + 16 * ((2 * s + h) ^ ((r >> 1) & 7));
        wa[i] = L_W + (4 * s + 2 * h) * 1024 + inner;
        PIN(xa[i]); PIN(wa[i]);
    }
    {
        const u32 c3 = (isx ? 2048 : 0) + ci * 128 + csw;
        vw3 = c3 < wrow3 ? (8 * w + l3) * wrow3 + c3 : OOB;
        v2 = (256 * hp + 8 * tg + l3) * 1024 + cb * 128 + 16 * ((l & 7) ^ (2 * (blk | ((w & 1) << 1))));
        sto = WS_ST + (rt * 128 + 32 * tg + r) * 16;
        PIN(vw3); PIN(v2); PIN(sto);
        const u32 f8 = (cb + 2 * (bi & 3)) & 7;
        f32k = f8 * 32768; f8k = f8 * 8192;
        t3[0] = f8 - 3 * ((f8 * 11) >> 5); t3[1] = t3[0] == 2 ? 0 : t3[0] + 1; t3[2] = 3 - t3[0] - t3[1];
        asm volatile("" : "+s"(f32k), "+s"(f8k), "+s"(t3[0]), "+s"(t3[1]), "+s"(t3[2]));
    }
    const u32 hm = (l >> 2) & 1;
    u32 int_a = 0x3f803f80u & (hm - 1), int_b = 0x3f803f80u & -hm, sidx = (l & 1) ? 0xeeee : 0x4444;
    u32x4 selu;
    { const u32 m = (((l >> 3) ^ h) & 1) - 1; selu = u32x4{int_a & m, int_a & m, int_b & m, int_b & m}; }
    u32 hmask = h - 1, csa = L_CS + (hp * 32 + r) * 16;
    u32 b1a = L_B1 + (hp * 32 + (l & 16) + 4 * ((hm << 1) | ((l >> 3) & 1)) + p) * 2;
    PIN(selu); PIN(sidx); PIN(hmask); PIN(csa); PIN(b1a);
    asm volatile("s_waitcnt lgkmcnt(0)" : "+s"(epoch));
    const u64 tag = epoch + 1;
    auto rowsums = [&]() {
        waitv<after<false>(ROW, 0, ST_AT + 1) - 1>(row);
        u32 ones = 0x3f803f80u;
        asm("" : "+s"(ones));
        float s = 0.f, sq = 0.f;
#pragma unroll
        for (int d = 0; d < 4; ++d) {
            const u32 rd = row[d];
            s = __builtin_amdgcn_fdot2_f32_bf16(__builtin_bit_cast(bf16x2, rd), __builtin_bit_cast(bf16x2, ones), s, false);
            sq = __builtin_amdgcn_fdot2_f32_bf16(__builtin_bit_cast(bf16x2, rd), __builtin_bit_cast(bf16x2, rd), sq, false);
        }
        const auto s32 = __builtin_amdgcn_permlane32_swap(__float_as_uint(s), __float_as_uint(sq), false, false);
        float v = __uint_as_float(s32[0]) + __uint_as_float(s32[1]);
        v = dpp_add<0x140>(dpp_add<0x141>(dpp_add<0x4e>(dpp_add<0xb1>(v))));
        const auto s16 = __builtin_amdgcn_permlane16_swap(__float_as_uint(v), __float_as_uint(v), false, false);
        v = __uint_as_float(s16[0]) + __uint_as_float(s16[1]);
        const auto sf = __builtin_amdgcn_permlane32_swap(__float_as_uint(v), __float_as_uint(v), false, false);
        st16c(u32x4{sf[0], sf[1], (u32)tag, (u32)(tag >> 32)}, rws, l == 0 ? WS_ST + srow * 16 : OOB);
    };
    if (ST_AT < 0) rowsums();
    STAMP(1); STAMPX(2, 5);

    u32x4 xf[32];
    f32x16 acc0 = {}, acc1 = {};
    float rs1, ga, gb, sdv;
    bf16x8 xfix;
    u32 bias2;
    u32x4 wp0, wp1;
    waitv_barrier<after<false>(T, 3, 0)>();
    STAMP(2); STAMPX(2, 6); STAMPX(1, 2);
    {
        bf16x8 wc[3][2], wf0, wf1;
        f32x4 cs = {}, cp0, cp1;
        u32 bias0, bias1;
        float mean, var, rs2;
        auto rdx = [&](int m) { xf[m] = L.r128(xa[m & 3] + ((m >> 2) & 3) * 16384); };
        auto rdw = [&](int m, int j) { wc[m % 3][j] = L.tr(wa[m & 3] + ((m >> 2) & 3) * 16384 + 512 * j, 1024); };
        rdx(0); rdw(0, 0); rdw(0, 1); rdx(1); rdw(1, 0); rdw(1, 1);
        ops(YT, IC<0>{});
        rep<0, 32>([&](auto N) {
            constexpr int n = N, t = n >> 2, i = n & 3;
            if constexpr (i == 3 && t < 7) {
                STAMPX(1, 3 + t);
                if constexpr (t == 5) { waitv_barrier<cmin(after<false>(T, 27, 1 + 2 * n), after<false>(SLD, 0, 1 + 2 * n))>(); PIN(st); }
                else waitv_barrier<after<false>(T, 4 * t + 7, 1 + 2 * n)>();
            }
            if constexpr (n == 30) { STAMPX(1, 10); waitv_barrier<after<false>(ECELL, 18, 61)>(); cp0 = L.r128f(csa); cp1 = L.r128f(csa + 1024); }
            SB();
            if constexpr (n == 28) { cs = SMFMA(asfrag(selu), cat(wc[n % 3][0], wc[n % 3][1]), cs, sidx); SB(); }
            acc0 = MFMA32(wc[n % 3][0], asfrag(xf[n]), acc0);
            PIN(acc0);
            SB();
            prio<3 - ((n + 1) & 3)>();
            if constexpr (i == 3 && t < 7) { rdx(n + 1); rdw(n + 1, 0); rdw(n + 1, 1); }
            else if constexpr (i != 2 && n < 30) { rdx(n + 2); rdw(n + 2, 0); }
            ops(YT, IC<1 + 2 * n>{});
            if constexpr (n == 24) {
                mean = __uint_as_float(st[0]) * (1.f / 512.f);
                var = fmaxf(__uint_as_float(st[1]) * (1.f / 512.f) - mean * mean, 0.f);
                rs1 = __builtin_amdgcn_rsqf(var + 1e-5f);
                PIN(rs1); PIN(var); PIN(mean);
            }
            if constexpr (n == 25) { sdv = __builtin_amdgcn_rcpf(rs2); ga = -2.f * 0.7978845608028654f * LOG2E * rs2; gb = ga * 0.044715f * rs2 * rs2; PIN(sdv); PIN(ga); PIN(gb); }
            if constexpr (n == 26) {
                const u32 hs = pk2(-mean, sdv);
                float lm = -mean - __uint_as_float(hs << 16); PIN(lm);
                const u32 ls = pk2(lm, sdv - __uint_as_float(hs & 0xffff0000u));
                u32x4 xu = {(hs & 0xffffu) | (ls << 16), hs, ls >> 16, 0u};
                PIN(xu);
                xfix = asfrag(xu);
            }
            if constexpr (n == 29) L.w32f(csa + h * 1024 + tg * 4, cs[0] + cs[1]);
            if constexpr (n == 31) wf1 = fix_operand(cp1, bias1, hmask);
            SB();
            acc1 = MFMA32(wc[n % 3][1], asfrag(xf[n]), acc1);
            PIN(acc1);
            SB();
            if constexpr (i == 3 && t < 7) { rdx(n + 2); rdw(n + 2, 0); rdw(n + 2, 1); }
            else if constexpr (i != 2 && n < 30) rdw(n + 2, 1);
            ops(YT, IC<2 + 2 * n>{});
            if constexpr (i == 2 && t < 7) cs = SMFMA(asfrag(selu), cat(wc[n % 3][0], wc[n % 3][1]), cs, sidx);
            if constexpr (2 + 2 * n == ST_AT) rowsums();
            if constexpr (n == 22) {
                const u32 b0 = L.r16(b1a), b1 = L.r16(b1a + 128);
                bias0 = b0 & (isx - 1); bias1 = b1 & (isx - 1); bias2 = b0 & -isx;
                PIN(bias0); PIN(bias1); PIN(bias2);
            }
            if constexpr (n == 23) poll16(st, sto, srdws, tag);
            if constexpr (n == 24) { rs2 = rs1 * __builtin_amdgcn_rsqf(var * rs1 * rs1 + 1e-5f); PIN(rs2); }
            if constexpr (n == 30) wf0 = fix_operand(cp0, bias0, hmask);
            SB();
        });
        acc0 = MFMA32(wf0, xfix, acc0);
        acc1 = MFMA32(wf1, xfix, acc1);
        PIN(acc0); PIN(acc1);
        SB();
    }
    STAMP(3); STAMPX(1, 11);
    auto phi_operand = [&]() {
        const u32x4 wpu = __builtin_bit_cast(u32x4, L.tr(L_SW + (2 * hp + (p & 1)) * 512 + (8 * h + q) * 32 + (2 * blk + (p >> 1)) * 8, 128));
        u32 m0 = hm - 1; PIN(m0);
        wp0 = wpu & m0; wp1 = wpu & ~m0;
        PIN(wp0); PIN(wp1);
    };
    auto logits = [&](const f32x16& a) { return MFMA32(asfrag(wp1), asfrag(pack8(a, 1)), MFMA32(asfrag(wp0), asfrag(pack8(a, 0)), f32x16{})); };
    u32 ba[4];
    auto phase_b_addresses = [&](int part) {
        if (part == 0) {
            hofs[0] = WS_H + ((prt * 172 + 32 * kq + g) * 128 + 64 * (rb & 1) + 16 * tg + tk) * 16; hofs[1] = hofs[0] ^ 256;
            PIN(hofs[0]); PIN(hofs[1]);
        } else if (part == 1) {
            const u32 swz = ((q >> 1) | (blk << 1)) ^ (2 * hp);
            ba[0] = 65536 + kq * 4096 + (8 * g + q) * 128 + 32 * swz + 8 * p;
            PIN(ba[0]);
        } else if (part == 2) {
#pragma unroll
            for (int c = 1; c < 4; ++c) { ba[c] = ba[0] ^ (32 * c); PIN(ba[c]); }
        } else if (part == 3) {
            flo[0] = WS_FG + (prt * 8 + ((3 * kq) >> 1) + (l & 1)) * 128; flo[1] = WS_FO + (prt * 8 + 4 * hp + p) * 128;
            PIN(flo[0]); PIN(flo[1]);
        } else if (part == 4) {
            colo = cb * 128 + 64 * hp + 32 * blk + 16 * h; outo = (rb * 64 + 16 * tg + tk) * 1024 + colo;
            PIN(colo); PIN(outo);
        } else if (part == 5) {
            oo = WS_O + ((prt * 64 + 32 * hp + g) * 128 + 64 * (rb & 1) + 16 * tg + tk) * 16;
            PIN(oo);
        }
    };
    auto pass2 = [&](auto XX, auto step, auto sync) {
        u32 wb[4];
#pragma unroll
        for (int i = 0; i < 4; ++i) { wb[i] = (4 * (i ^ rot) + 2 * h) * 512 + inner; PIN(wb[i]); }
        bf16x8 f[3], c0, c1, wf;
        f32x16 acc = {};
        f32x4 cs = {}, cp;
        sync(IC<0>{});
        f[0] = L.tr(wb[0], 512); f[1] = L.tr(wb[1], 512);
        u32x4 sel2 = {int_a, int_a, int_b, int_b};
        PIN(sel2);
        rep<0, 32>([&](auto N) {
            constexpr int n = N;
            if constexpr (n == MIDB || n == 30) sync(N);
            SB();
            acc = MFMA32(f[n % 3], asfrag(xf[n]), acc);
            PIN(acc);
            SB();
            if constexpr (((n + 2) & 3) == 0) prio<3 - (((n + 2) & 15) >> 2)>();
            if constexpr (n + 2 < 32) f[(n + 2) % 3] = L.tr(wb[(n + 2) & 3] + 8192 * ((n + 2) >> 2), 512);
            if constexpr (n >= 2 && n <= 8 && !(n & 1)) { cs = SMFMA(asfrag(sel2), cat(c0, c1), cs, sidx); PIN(cs); SB(); }
            if constexpr (n <= 6 && !(n & 1)) { c0 = L.tr(wb[0] + 8192 * n, 512); c1 = L.tr(wb[0] + 8192 * (n + 1), 512); }
            if constexpr (n == 11) L.w32f(csa + 2048 + tg * 4, cs[0] + cs[1]);
            if constexpr (n == MIDB) cp = L.r128f(csa + 2048);
            if constexpr (n == MIDB + 2) wf = fix_operand(cp, XX ? bias2 : 0, hmask);
            if constexpr (n == MIDB + 4) { acc = MFMA32(wf, xfix, acc); PIN(acc); }
            ops(XX, IC<200 + n>{});
            step(N);
            SB();
        });
        return acc;
    };
    auto gelu2 = [&](float a, float c) {
        const f32x2 gap = {ga, ga}, gbp = {gb, gb}, sdp = {sdv, sdv}, v = {a, c};
        const f32x2 z = v * __builtin_elementwise_fma(v * v, gbp, gap);
        const f32x2 e = {__builtin_amdgcn_exp2f(z[0]), __builtin_amdgcn_exp2f(z[1])};
        const f32x2 d = __builtin_elementwise_fma(e, sdp, sdp);
        const f32x2 y = v * f32x2{__builtin_amdgcn_rcpf(d[0]), __builtin_amdgcn_rcpf(d[1])};
        return pk2(y[0], y[1]);
    };

    auto phase_b = [&](auto XX) {
        constexpr bool X = XX;
        constexpr int SG = X ? X_FG : Y_FG, SO = X ? X_FO : Y_FO;
        f32x4 am[2][4], as[4];
#pragma unroll
        for (int c = 0; c < 4; ++c) { am[0][c] = f32x4{}; am[1][c] = f32x4{}; as[c] = f32x4{}; }
        u32 wl = w1k + l16, wl2 = wl + 65536, wl3 = wl + L_SCR, bb[4];
        s16x4 f[2][8];
#pragma unroll
        for (int m = 0; m < 8; ++m) f[0][m] = L.trh(ba[m >> 1] + 512 * (m & 1));
        PIN(wl); PIN(wl2); PIN(wl3);
        rep<0, 11>([&](auto S) {
            constexpr int s = S, n = s + 1, at = 400 + 8 * s;
            constexpr int wh = after<X>(HL, 2 * s + 1, at);
            if constexpr (s == 4) {
                waitv_barrier<X ? cmin(wh, after<X>(CELL, 7, at)) : wh>();
                flag<1>(w, tag, srdws, (X ? WS_FG : WS_FO) + (rt * 8 + ci) * 128);
                rep<0, 11>([&](auto J) { PIN(raw[J]); });
                STAMP(9);
            } else if constexpr (X && s == 7) waitv_barrier<cmin(wh, after<X>(CELL, 18, at))>();
            else if constexpr (s == 8) waitv_barrier<wh>();
            if constexpr (s == SG) poll<after<X>(FL, 0, at)>(fl, flo[0], srdws, tag);
            if constexpr (s == SO) poll<after<X>(FL, 1, at)>(fl, flo[1], srdws, tag);
            waitv<wh>(hf[2 * s], hf[2 * s + 1]);
            rep<0, 8>([&](auto M) {
                constexpr int m = M, c = m >> 1;
                SB();
                am[m & 1][c] = MFMA16(Lds::join(f[s & 1][2 * c], f[s & 1][2 * c + 1]), asfrag(hf[2 * s + (m & 1)]), am[m & 1][c]);
                PIN(am[m & 1][c]);
                SB();
                if constexpr (m == 0) prio<3 - (s & 3)>();
                if constexpr (n < 11) {
                    if constexpr (!(m & 1) && (n == 4 || n == 8)) { if constexpr (n == 4) { bb[c] = ba[c] - 65536; PIN(bb[c]); } else { ba[c] += 65536; PIN(ba[c]); } }
                    f[n & 1][m] = L.trh((n < 4 || n == 8 || n == 9 ? ba[c] : bb[c]) + (n == 10 ? 0 : (n & 3) * 16384) + 512 * (m & 1));
                }
                ops(XX, IC<at + m>{});
                constexpr int j = 3 * (s - 4) + (m - 2) / 2;
                if constexpr (s >= 4 && m >= 2 && (m & 1) && j < 11) L.w128(j == 0 ? wl3 : (j < 3 ? wl + (j - 1) * 8192 : wl2 + (j - 3) * 8192), raw[j]);
            });
        });
        SB();
        STAMP(10);
        if constexpr (SO == 11) poll<after<X>(FL, 1, 500)>(fl, flo[1], srdws, tag);
        ops(XX, IC<500>{});
        STAMP(11);
        u32 xo = L_XM + w2k + l16;
        PIN(xo);
#pragma unroll
        for (int k = 1; k < 4; ++k) {
            L.w128f(xo + (k - 1) * 16384, am[k >> 1][2 * (k & 1)]);
            L.w128f(xo + (k - 1) * 16384 + 1024, am[k >> 1][2 * (k & 1) + 1]);
        }
        bf16x8 wf[3][4];
#pragma unroll
        for (int c = 0; c < 4; ++c) { bb[c] += 65536 - (tg & 1) * 8192; PIN(bb[c]); }
        waitv<after<X>(BR, 0, 501)>(xr, br);
        const auto b0 = __builtin_amdgcn_permlane16_swap(br[0], br[2], false, false), b1 = __builtin_amdgcn_permlane16_swap(br[1], br[3], false, false);
        const auto x0 = __builtin_amdgcn_permlane16_swap(xr[0], xr[2], false, false), x1 = __builtin_amdgcn_permlane16_swap(xr[1], xr[3], false, false);
        u32 xres[4] = {x0[0], x1[0], x0[1], x1[1]}, xi[3] = {xo ^ 8192, xo ^ 2048, xo ^ 10240};
        f32x4 mm[2];
#pragma unroll
        for (int j = 0; j < 2; ++j) {
            mm[j] = am[0][j] + f32x4{__uint_as_float(b0[j] << 16), __uint_as_float(b0[j] & 0xffff0000u), __uint_as_float(b1[j] << 16), __uint_as_float(b1[j] & 0xffff0000u)};
            PIN(mm[j]); PIN(xres[2 * j]); PIN(xres[2 * j + 1]);
        }
        PIN(xi[0]); PIN(xi[1]); PIN(xi[2]);
        BARRIER();
        f32x4 gate[2];
        {
            f32x4 pm[2][3];
#pragma unroll
            for (int j = 0; j < 2; ++j)
#pragma unroll
                for (int k = 0; k < 3; ++k) pm[j][k] = L.r128f(xi[k] + k * 16384 + j * 1024);
            SB();
#pragma unroll
            for (int j = 0; j < 2; ++j) {
#pragma unroll
                for (int c = 0; c < 4; ++c) wf[j][c] = L.tr(bb[c] + j * 8192, 512);
                const f32x4 m = (mm[j] + pm[j][0]) + (pm[j][1] + pm[j][2]), z = m * -LOG2E;
                const f32x4 d = f32x4{__builtin_amdgcn_exp2f(z[0]), __builtin_amdgcn_exp2f(z[1]), __builtin_amdgcn_exp2f(z[2]), __builtin_amdgcn_exp2f(z[3])} + 1.f;
                gate[j] = m * f32x4{__builtin_amdgcn_rcpf(d[0]), __builtin_amdgcn_rcpf(d[1]), __builtin_amdgcn_rcpf(d[2]), __builtin_amdgcn_rcpf(d[3])};
                PIN(gate[j]);
                SB();
            }
        }
        u32 xs = L_XS + w2k + l16;
        PIN(xs);
        rep<0, 8>([&](auto J) {
            constexpr int j = J;
            waitv<after<X>(OL, j, 501)>(of[j]);
            rep<0, 4>([&](auto C) {
                constexpr int c = j == 7 ? (C + 2) & 3 : C;
                SB();
                as[c] = MFMA16(wf[j % 3][c], asfrag(of[j]), as[c]);
                PIN(as[c]);
                SB();
                if constexpr (C == 0 && !(j & 1)) prio<3 - (j >> 1)>();
                if constexpr (j < 6) wf[(j + 2) % 3][c] = L.tr(bb[c] + (j + 2) * 8192, 512);
            });
        });
        L.w128f(xs, as[2]);
        L.w128f(xs + 1024, as[3]);
        xs ^= 8192;
        f32x4 y0[2];
#pragma unroll
        for (int j = 0; j < 2; ++j) {
            y0[j] = __builtin_elementwise_fma(as[j], gate[j], f32x4{__uint_as_float(xres[2 * j] << 16), __uint_as_float(xres[2 * j] & 0xffff0000u),
                                                                     __uint_as_float(xres[2 * j + 1] << 16), __uint_as_float(xres[2 * j + 1] & 0xffff0000u)});
            PIN(y0[j]);
        }
        BARRIER();
        STAMP(12);
        {
            u32 y[4];
#pragma unroll
            for (int j = 0; j < 2; ++j) {
                const f32x4 v = __builtin_elementwise_fma(L.r128f(xs + j * 1024), gate[j], y0[j]);
                y[2 * j] = pk2(v[0], v[1]); y[2 * j + 1] = pk2(v[2], v[3]);
            }
            const auto s0 = __builtin_amdgcn_permlane16_swap(y[0], y[2], false, false), s1 = __builtin_amdgcn_permlane16_swap(y[1], y[3], false, false);
            const rsrc_t ro = __builtin_amdgcn_make_buffer_rsrc((void*)((u64)kb[2] | ((u64)kb[3] << 32)), 0, 2048 * 1024, 0x00020000);
            __builtin_amdgcn_raw_buffer_store_b128(u32x4{s0[0], s1[0], s0[1], s1[1]}, ro, outo, 0, 0);
        }
    };

    if (isx) {
        {
            phi_operand();
            u32 va = L_V + (32 * tg + r) * 128 + 16 * ((4 * hp + h) ^ (r & 7));
            u32 pa = L_PHI + ((2 * hp + h) * 128 + 32 * tg + r) * 64 + 16 * ((r >> 1) & 3);
            PIN(va); PIN(pa);
            SB();
            const f32x16 pk = logits(acc0);
            acc1 *= rs1;
            L.w128(va, pack8(acc1, 0));
            L.w128(va ^ 32, pack8(acc1, 1));
            f32x2 ep[8], en[8], sp, sn;
            dual_exp(pk, rs1, ep, en, [&](auto I) {
                if constexpr (I == 0) { sp = ep[0]; sn = en[0]; } else { sp += ep[I]; sn += en[I]; }
                if constexpr (I & 1) { SB(); ops(XT, IC<102 + I / 2>{}); }
            });
            const float rp = __builtin_amdgcn_rcpf(sp[0] + sp[1]), rn = __builtin_amdgcn_rcpf(sn[0] + sn[1]);
#pragma unroll
            for (int c = 0; c < 4; ++c) {
                u32x4 o;
#pragma unroll
                for (int e = 0; e < 4; ++e) {
                    const int i = (c & 2) + (e & 1) + 2 * (e & 2);
                    const f32x2 a = (c & 1) ? en[i] * rn : ep[i] * rp;
                    o[e] = pk2(a[0], a[1]);
                }
                L.w128(pa ^ (16 * c), o);
            }
        }
        {
            u32 a0 = L_PHI + (tg * 128 + 4 * g + q) * 64 + 16 * ((p >> 1) ^ (2 * blk + (q >> 1))) + 8 * (p & 1), a1 = a0 ^ 32;
            u32 b0 = L_V + (4 * g + q) * 128 + 16 * ((2 * tg + (p >> 1)) ^ (4 * blk + q)) + 8 * (p & 1);
            u32 ko = WS_PKV + (ci * 16 + rt) * REC + (hp ? (tk == 0 ? 4096 + tg * 64 + l : OOB) : tg * 1024 + l16);
            PIN(a0); PIN(a1); PIN(b0); PIN(ko);
            waitv_barrier<after<true>(W3, 7, 150)>();
            STAMP(4);
            bf16x8 af[4][2], vf[4];
#pragma unroll
            for (int s = 0; s < 4; ++s) { af[s][0] = L.tr(a0 + s * 2048, 1024); vf[s] = L.tr(b0 + s * 4096, 2048); af[s][1] = L.tr(a1 + s * 2048, 1024); }
            if (hp) {
#pragma unroll
                for (int s = 0; s < 4; ++s) vf[s] = asfrag(u32x4{0x3f803f80u, 0x3f803f80u, 0x3f803f80u, 0x3f803f80u});
            }
            f32x4 k0 = {}, k1 = {};
#pragma unroll
            for (int s = 0; s < 4; ++s) { k0 = MFMA16(af[s][0], vf[s], k0); k1 = MFMA16(af[s][1], vf[s], k1); }
            st16c(u32x4{pk2(k0[0], k0[1]), pk2(k0[2], k0[3]), pk2(k1[0], k1[1]), pk2(k1[2], k1[3])}, rws, ko);
            SB();
        }
        STAMP(5);
        u32 fo, h2;
        const f32x16 acc2 = pass2(XT, [&](auto N) {
            constexpr int n = N;
            if constexpr (n == 9) { fo = WS_FH + (prt * 8 + (l & 7)) * 128; PIN(fo); }
            if constexpr (n == 10) { h2 = (2 * ci + hp < 11) ? WS_H + ((rt * 172 + 128 + 8 * ci + 4 * hp + h) * 128 + 32 * tg + r) * 16 : OOB; PIN(h2); }
            if constexpr (n >= 20 && n < 26) phase_b_addresses(n - 20);
            if constexpr (n == 27) ld8c_w0(fl, w, fo, srdws);
        }, [&](auto N) {
            if constexpr (N == MIDB) {
                waitv_barrier<after<true>(KST, 0, 200 + N)>();
                flag<1>(w, tag, srdws, WS_FP + (ci * 16 + rt) * 128);
                STAMP(6);
            } else if constexpr (N == 30) {
                poll_w0<1>(fl, w, fo, srdws, tag);
                BARRIER();
            }
        });
        STAMP(7);
        u32x4 o[2];
        rep<0, 8>([&](auto I) {
            ops(XT, IC<300 + I>{});
            o[I >> 2][I & 3] = gelu2(acc2[2 * I], acc2[2 * I + 1]);
            if constexpr (I & 1) SB();
        });
        st16c(o[0], rws, h2);
        st16c(o[1], rws, h2 + 4096);
        SB();
        ops(XT, IC<310>{});
        waitv_barrier<after<true>(CELL, 3, 311)>();
        STAMP(8);
        phase_b(XT);
#pragma unroll
        for (int n = 0; n < 32; ++n) asm volatile("" : "=v"(xf[n]));
        asm volatile("" : "=v"(acc0), "=v"(acc1), "=v"(xfix));
    }
    u32 isx2 = isx;
    asm volatile("" : "+s"(isx2));
    if (!isx2) {
        {
            u32 ho = WS_H + ((rt * 172 + 16 * ci + 4 * hp + h) * 128 + 32 * tg + r) * 16;
            PIN(ho);
            if (w == 2) { const u32 v = (l >> 1) == 1 ? 0x3f803f80u : 0u; L.w128(L_ZERO + l16, u32x4{v, v, v, v}); }
            u32x4 o;
            rep<0, 16>([&](auto I) {
                constexpr int i = I;
                ops(YT, IC<100 + i>{});
                o[i & 3] = i < 8 ? gelu2(acc0[2 * (i & 7)], acc0[2 * (i & 7) + 1]) : gelu2(acc1[2 * (i & 7)], acc1[2 * (i & 7) + 1]);
                if constexpr ((i & 3) == 3) st16c(o, rws, ho + (i >> 3) * 16384 + ((i >> 2) & 1) * 4096);
                if constexpr (i & 1) SB();
            });
        }
        STAMP(4);
        u32 fo, kvw, fa, dz0, dz1, os;
        u32x4 one;
        const f32x16 acc2 = pass2(YT, [&](auto N) {
            constexpr int n = N;
            if constexpr (n == 9) { fo = blk ? WS_FH + (prt * 8 + (l & 7)) * 128 : WS_FP + (ci * 16 + tk) * 128; PIN(fo); }
            if constexpr (n == 10) { go = WS_PKV + (ci * 16 + 8 * h) * REC + (32 * w + r) * 16; PIN(go); }
            if constexpr (n == 12) phi_operand();
            if constexpr (n == 13) { kvw = L_KV + (32 * w + 16 * h + tk) * 16 + 8 * blk; PIN(kvw); }
            if constexpr (n == 15) { fa = hm == h ? L_KV + (2 * hp + h) * 1024 + (p + 4 * (r >> 3)) * 16 : L_ZERO; PIN(fa); }
            if constexpr (n == 17) {
                const u32 z = L_ZERO + ((p & 2) ? 32 : 128 + (2 * hp + h) * 256 + 32 * (p & 1));
                dz0 = (q == g && !(p & 1)) ? z : L_ZERO; dz1 = (q == g && (p & 1)) ? z : L_ZERO;
                PIN(dz0); PIN(dz1);
            }
            if constexpr (n == 19) {
                const u64 v = (u64)(((l >> 3) ^ (l >> 4)) & 1 ? 0u : 0x3f80u) << (16 * p);
                one = u32x4{(u32)v & (hm - 1), (u32)(v >> 32) & (hm - 1), (u32)v & -hm, (u32)(v >> 32) & -hm};
                PIN(one);
            }
            if constexpr (n >= 20 && n < 26) phase_b_addresses(n - 20);
            if constexpr (n == 26) { ld8c_w0(fl, w, fo, srdws); os = WS_O + ((rt * 64 + 2 * (4 * ci + 2 * hp + h)) * 128 + 32 * tg + r) * 16; PIN(os); }
        }, [&](auto N) {
            if constexpr (N == 0) waitv_barrier<after<false>(W3, 7, 200)>();
            else if constexpr (N == MIDB) {
                waitv_barrier<after<false>(HST, 3, 200 + N)>();
                flag<1>(w, tag, srdws, WS_FH + (rt * 8 + ci) * 128);
                STAMP(5);
            } else {
                poll_w0<0>(fl, w, fo, srdws, tag);
                BARRIER();
                STAMP(6);
                if (w == 7) {
                    const u32 zo = WS_PKV + (ci * 16 + g) * REC + 4096 + tk * 16;
#pragma unroll
                    for (int m = 0; m < 4; ++m) ld16c(of[m], zo, srdws, 4 * m * REC);
                }
            }
        });
        u32x4 pq[4];
        {
            ops(YT, IC<300>{});
            asm volatile("s_waitcnt vmcnt(%4)" : "+v"(raw[11]), "+v"(raw[12]), "+v"(raw[13]), "+v"(raw[14]) : "n"(after<false>(RAW, 14, 301)) : "memory");
            u32 wl = w1k + l16; PIN(wl);
            L.w128(wl + 4 * 8192, raw[11]); L.w128(wl + 5 * 8192, raw[12]);
            SB();
            f32x16 pp = logits(acc2);
            PIN(pp);
            SB();
            ops(YT, IC<301>{});
            L.w128(wl + 6 * 8192, raw[13]); L.w128(wl + 7 * 8192, raw[14]);
            SB();
            f32x2 ep[8], en[8];
            dual_exp(pp, rs1, ep, en, [&](auto) {});
#pragma unroll
            for (int i = 0; i < 8; ++i) { pq[i >> 2][i & 3] = pk2(ep[i][0], ep[i][1]); pq[2 + (i >> 2)][i & 3] = pk2(en[i][0], en[i][1]); }
#pragma unroll
            for (int i = 0; i < 4; ++i) PIN(pq[i]);
            SB();
        }
        STAMP(7);
        {
            f32x4 sm = {};
            rep<0, 8>([&](auto J) {
                waitv<after<false>(GA, J, 310 + J)>(gt[J]);
                sm = MFMA16(asfrag(one), asfrag(gt[J]), sm);
                PIN(sm);
                SB();
                ops(YT, IC<310 + J>{});
                SB();
            });
            if (w == 7) {
                const u64 v = (u64)0x3f80u << (16 * p);
                const u32x4 all = {(u32)v & (hm - 1), (u32)(v >> 32) & (hm - 1), (u32)v & -hm, (u32)(v >> 32) & -hm};
                f32x4 sz = {};
#pragma unroll
                for (int m = 0; m < 4; ++m) { PIN(of[m]); sz = MFMA16(asfrag(all), asfrag(of[m]), sz); }
                L.w64(L_ZERO + 128 + q * 256 + p * 16 + 8 * blk, u32x2{pk2(sz[0], sz[1]), pk2(sz[2], sz[3])});
            }
            L.w64(kvw, u32x2{pk2(sm[0], sm[1]), pk2(sm[2], sm[3])});
        }
        BARRIER();
        {
            u32x4 zf[4], kf[4];
#pragma unroll
            for (int s = 0; s < 4; ++s) zf[s] = L.r128((s < 2 ? dz0 : dz1) + (s & 1) * 16);
#pragma unroll
            for (int s = 0; s < 4; ++s) kf[s] = L.r128(fa + s * 256);
            SB();
            f32x4 dn = {};
#pragma unroll
            for (int s = 0; s < 4; ++s) dn = MFMA16(asfrag(zf[s]), asfrag(pq[s]), dn);
            PIN(dn);
            SB();
            f32x16 np = MFMA32(asfrag(kf[1]), asfrag(pq[1]), MFMA32(asfrag(kf[0]), asfrag(pq[0]), f32x16{}));
            f32x16 nn = MFMA32(asfrag(kf[2]), asfrag(pq[2]), f32x16{});
            PIN(np); PIN(nn);
            SB();
            const float rd = __builtin_amdgcn_rcpf(fmaf(dn[3], dn[0], dn[2] * fmaf(dn[3], 1e-6f, dn[1])));
            float cp = dn[3] * rd; PIN(cp);
            float cn = dn[2] * rd; PIN(cn);
            SB();
            nn = MFMA32(asfrag(kf[3]), asfrag(pq[3]), nn);
            const f32x16 o = __builtin_elementwise_fma(nn, f32x16{} + cn, np * cp);
            st16c(pack8(o, 0), rws, os);
            st16c(pack8(o, 1), rws, os + 2048);
            SB();
            asm volatile("s_nop 0");
            SB();
        }
        STAMP(8);
        phase_b(YT);
    }
    if (b == 0 && w == 0) {
        poll<0>(~tag, WS_FH + l * 128, srdws, tag);
        poll<0>(~tag, WS_FH + (64 + l) * 128, srdws, tag);
        flag<0>(w, tag, srdws, 0);
    }
    asm volatile(".Lpf_end:");
    asm volatile("s_waitcnt vmcnt(0)" ::: "memory");
    STAMP(13);
    if (threadIdx.x == 64 * PROFW) {
        u64* sp = (u64*)((char*)ws_ + WS_PROF) + ((tag & 15) * 256 + b) * 16;
        stamps[14] = !isx;
#pragma unroll
        for (int i = 0; i < 16; ++i) sp[i] = stamps[i];
    }
}

extern "C" void kernel_launch(void* const* d_in, const int* in_sizes, int n_in, void* d_out, int out_size, void* d_ws, size_t ws_size,
                              hipStream_t stream) {
    hedgehog_mixer_kernel<<<256, 512, 0, stream>>>(d_in[0], d_in[1], d_in[5], d_ws, d_in[7], d_in[3], d_in[2], d_in[6], d_in[8], d_out);
}
```

```cpp
#include <hip/hip_runtime.h>
#include <hip/hip_bf16.h>
#include <type_traits>

typedef unsigned u32;
typedef unsigned long long u64;
typedef __bf16 bf16x2 __attribute__((ext_vector_type(2)));
typedef __bf16 bf16x8 __attribute__((ext_vector_type(8)));
typedef __bf16 bf16x16 __attribute__((ext_vector_type(16)));
typedef short s16x4 __attribute__((ext_vector_type(4)));
typedef float f32x2 __attribute__((ext_vector_type(2)));
typedef float f32x4 __attribute__((ext_vector_type(4)));
typedef float f32x16 __attribute__((ext_vector_type(16)));
typedef u32 u32x2 __attribute__((ext_vector_type(2)));
typedef u32 u32x4 __attribute__((ext_vector_type(4)));
typedef u32 u32x8 __attribute__((ext_vector_type(8)));
typedef u32 u32x16 __attribute__((ext_vector_type(16)));
#define LAS __attribute__((address_space(3)))
#define DI __device__ __forceinline__
#define SB() __builtin_amdgcn_sched_barrier(0)
template <int N> using IC = std::integral_constant<int, N>;
template <int A, int B, class F> DI void rep(F f) { if constexpr (A < B) { f(IC<A>{}); rep<A + 1, B>(f); } }

#define PROF 0
#define PROFW 0
#define ROTMODE 11
#define PRIO 1
#define SKIP 0
#define SUMI 1
#define EPW (71 * 8 + 2)
#define OG 4
#define ZWV 3
#define MAP 1
#define RHALF ((xcd ^ (xcd >> 2)) & 1)
#define CGRP (xcd & 3)
#define NTX 0
#define NTW 0
#define NT2 0
#define NT3 0

constexpr u32 WS_FP = 4096;
constexpr u32 WS_FH = 16384;
constexpr u32 WS_FG = 32768;
constexpr u32 WS_FO = 49152;
constexpr u32 WS_PZ = 98304;
constexpr u32 WS_PKV = 131072;
constexpr u32 WS_O = WS_PKV + 8 * 65536;
constexpr u32 HRT = 6 * 65536;
constexpr u32 WS_H = WS_O + 16 * 131072;
constexpr u32 WS_END = WS_H + 16 * HRT;
constexpr u32 WS_PROF = 16u << 20;
constexpr u32 OOB = 0x7ff00000u;

constexpr u32 L_W = 65536;
constexpr u32 L_V = 2 * 8192;
constexpr u32 L_PHI = 4 * 8192;
constexpr u32 L_SCR = 19 * 8192;
constexpr u32 L_B1 = L_SCR, L_B2 = L_SCR + 256, L_DUMP = L_SCR + 512;
constexpr u32 L_CS = L_SCR + 1024;
constexpr u32 L_SW = L_SCR + 4096;
constexpr u32 L_KV = L_SCR + 2048;
constexpr u32 L_ONE = L_SCR + 6144;
constexpr u32 L_ZERO = L_SCR + 7168;
constexpr u32 L_XM = 10 * 8192;
constexpr u32 L_XS = 16 * 8192;

constexpr float LOG2E = 1.4426950408889634f;

enum { T, MISC, SWL, W3, CELL, RAW, LD, GA, HL, FL, XR, OL,   FW, EL, ZW,   SU, PL, EP, LW,   CLR, HST, KST, OST };
struct Op { int at, kind, arg; };
constexpr int MIDB = 16, XMIDB = 14, XB = 4, BA = 4, B0 = 0, FPAT = 26, XFG = 16, YFG = 32;
constexpr Op YOPS[] = {
    {-3, T, 0}, {-3, T, 1}, {-3, T, 2}, {-3, T, 3}, {-2, T, 4}, {-2, T, 5}, {-2, T, 6}, {-2, T, 7}, {-1, SU, 1}, {-1, SU, 2},
    {-1, SU, 3}, {-1, SU, 0}, {-1, MISC, 0}, {-1, SWL, 0}, {-1, T, 8}, {-1, T, 9}, {-1, T, 10}, {-1, T, 11}, {3, SU, 4}, {5, SU, 6},
    {5, T, 12}, {6, T, 13}, {7, SU, 5}, {7, T, 14}, {8, T, 15}, {13, T, 16}, {14, T, 17}, {15, T, 18}, {16, T, 19}, {19, T, 20},
    {20, T, 21}, {22, T, 22}, {23, T, 23}, {25, T, 24}, {26, T, 25}, {28, T, 26}, {29, T, 27}, {31, T, 28}, {32, T, 29}, {34, T, 30},
    {35, T, 31}, {37, MISC, 15}, {40, W3, 0}, {42, W3, 1}, {46, W3, 2}, {48, W3, 3}, {59, W3, 4}, {60, W3, 5}, {62, W3, 6}, {63, W3, 7},
    {101, CELL, 0}, {102, CELL, 1}, {103, HST, 0}, {105, CELL, 2}, {106, CELL, 3}, {107, HST, 1}, {109, CELL, 4}, {111, HST, 2}, {113, CELL, 5}, {115, HST, 3},
    {201, CELL, 6}, {203, CELL, 7}, {205, RAW, 12}, {207, RAW, 13}, {208, FW, 0}, {209, RAW, 14}, {211, RAW, 15}, {213, CELL, 16}, {214, CELL, 17}, {216, CELL, 8},
    {217, CELL, 9}, {219, ZW, 0}, {219, GA, 0}, {220, GA, 1}, {222, GA, 2}, {223, GA, 3}, {225, GA, 4}, {226, GA, 5}, {228, GA, 6}, {229, GA, 7},
    {231, FW, 1}, {310, CELL, 10}, {311, CELL, 11}, {312, CELL, 18}, {313, RAW, 20}, {314, RAW, 21}, {315, RAW, 22}, {316, RAW, 23}, {317, RAW, 24}, {320, HL, 0},
    {320, HL, 1}, {326, HL, 2}, {327, HL, 3}, {328, HL, 4}, {331, HL, 5}, {332, HL, 6}, {333, HL, 7}, {334, HL, 8}, {335, HL, 9}, {340, OST, 0},
    {340, OST, 1}, {403, LW, 12}, {409, HL, 10}, {411, LW, 13}, {412, HL, 11}, {416, HL, 12}, {419, LW, 14}, {420, HL, 13}, {424, HL, 14}, {427, LW, 15},
    {428, HL, 15}, {432, FL, 2}, {432, LD, 25}, {434, LW, 20}, {435, LD, 26}, {438, LW, 21}, {439, LD, 27}, {440, PL, 2}, {442, LW, 22}, {443, HL, 16},
    {446, LW, 23}, {447, HL, 17}, {450, HL, 18}, {450, LW, 24}, {454, HL, 19}, {458, HL, 20}, {459, HL, 21}, {466, LD, 28}, {470, FL, 3}, {470, LD, 29},
    {473, LD, 30}, {476, PL, 3}, {477, OL, 0}, {481, OL, 1}, {485, OL, 2}, {490, OL, 3}, {490, OL, 4}, {491, OL, 5}, {492, OL, 6}, {493, OL, 7},
    {494, XR, 0},
};
constexpr Op XOPS[] = {
    {-3, T, 0}, {-3, T, 1}, {-3, T, 2}, {-3, T, 3}, {-2, T, 4}, {-2, T, 5}, {-2, T, 6}, {-2, T, 7}, {-1, SU, 1}, {-1, SU, 2},
    {-1, SU, 3}, {-1, SU, 0}, {-1, MISC, 0}, {-1, SWL, 0}, {-1, T, 8}, {-1, T, 9}, {-1, T, 10}, {-1, T, 11}, {3, SU, 4}, {5, SU, 6},
    {5, T, 12}, {6, T, 13}, {7, SU, 5}, {7, T, 14}, {8, T, 15}, {9, SU, 7}, {11, SU, 8}, {13, T, 16}, {14, T, 17}, {15, T, 18},
    {16, T, 19}, {19, T, 20}, {20, T, 21}, {22, T, 22}, {23, T, 23}, {25, T, 24}, {26, T, 25}, {28, T, 26}, {29, T, 27}, {31, T, 28},
    {32, T, 29}, {34, T, 30}, {35, T, 31}, {36, CLR, 0}, {37, MISC, 15}, {40, W3, 0}, {42, W3, 1}, {46, W3, 2}, {48, W3, 3}, {59, W3, 4},
    {60, W3, 5}, {62, W3, 6}, {63, W3, 7}, {101, CELL, 0}, {102, CELL, 1}, {104, RAW, 12}, {106, RAW, 13}, {107, RAW, 14}, {150, KST, 0}, {201, RAW, 15},
    {202, CELL, 16}, {204, CELL, 17}, {205, CELL, 18}, {207, RAW, 20}, {208, RAW, 21}, {210, RAW, 22}, {211, RAW, 23}, {213, RAW, 24}, {214, CELL, 2}, {217, CELL, 3},
    {219, CELL, 4}, {221, CELL, 5}, {223, CELL, 6}, {225, CELL, 7}, {226, FL, 1}, {227, CELL, 8}, {229, CELL, 9}, {231, CELL, 10}, {300, PL, 1}, {300, HL, 0},
    {301, HL, 1}, {302, HL, 2}, {303, HL, 3}, {303, HST, 0}, {306, HL, 4}, {307, HL, 5}, {307, HST, 1}, {308, HL, 6}, {400, HL, 7}, {403, CELL, 11},
    {403, LW, 12}, {407, HL, 8}, {410, EL, 0}, {411, HL, 9}, {411, LW, 13}, {414, HL, 10}, {418, HL, 11}, {419, LW, 14}, {422, RAW, 25}, {422, RAW, 26},
    {427, LW, 15}, {430, HL, 12}, {433, HL, 13}, {434, LW, 20}, {436, FL, 2}, {437, HL, 14}, {438, LW, 21}, {441, HL, 15}, {442, LW, 22}, {444, PL, 2},
    {445, HL, 16}, {446, LW, 23}, {449, HL, 17}, {450, EP, 0}, {450, LW, 24}, {452, HL, 18}, {454, LW, 25}, {456, HL, 19}, {458, HL, 20}, {458, LW, 26},
    {459, HL, 21}, {468, LD, 27}, {472, LD, 28}, {475, LD, 29}, {478, LD, 30}, {482, FL, 3}, {488, PL, 3}, {488, OL, 0}, {488, OL, 1}, {490, OL, 2},
    {490, OL, 3}, {492, OL, 4}, {492, OL, 5}, {494, OL, 6}, {494, OL, 7}, {494, XR, 0},
};
template <bool X> constexpr int nops() { return X ? sizeof(XOPS) / sizeof(Op) : sizeof(YOPS) / sizeof(Op); }
template <bool X> constexpr Op opat(int i) { return X ? XOPS[i] : YOPS[i]; }
constexpr int cmin(int a, int b) { return a < b ? a : b; }
template <bool X> constexpr int after(int kind, int lo, int hi, int slot) {
    int me = -1, c = 0;
    for (int i = 0; i < nops<X>(); ++i) if (opat<X>(i).kind == kind && opat<X>(i).arg >= lo && opat<X>(i).arg <= hi) me = i;
    if (me < 0) return 63;
    if (opat<X>(me).at >= slot) return -1;
    for (int i = me + 1; i < nops<X>(); ++i) if (opat<X>(i).at < slot && (opat<X>(i).kind < FW || opat<X>(i).kind > LW)) ++c;
    return cmin(c, 63);
}
template <bool X> constexpr int after(int kind, int arg, int slot) { return after<X>(kind, arg, arg, slot); }
template <bool X> constexpr int nth(int slot, int j) {
    int c = 0;
    for (int i = 0; i < nops<X>(); ++i) if (opat<X>(i).at == slot && opat<X>(i).kind < CLR && c++ == j) return i;
    return -1 - c;
}
template <bool X> constexpr int slot_of(int kind, int arg) {
    for (int i = 0; i < nops<X>(); ++i) if (opat<X>(i).kind == kind && opat<X>(i).arg == arg) return opat<X>(i).at;
    return -99;
}

DI u32x4 mk_srd(u32 lo, u32 hi, u32 bytes) { return u32x4{lo, hi & 0xffffu, bytes, 0x00020000u}; }
template <u32 C> DI u32 seq(u32 x, u32 a, u32 b) {
    u32 r;
    asm("s_cmp_eq_u32 %1, %2\n\ts_cselect_b32 %0, %3, %4" : "=s"(r) : "s"(x), "n"(C), "s"(a), "s"(b) : "scc");
    return r;
}
template <u32 M> DI u32 sbit(u32 x, u32 a) {
    u32 r;
    asm volatile("s_bitcmp1_b32 %2, %1\n\ts_cselect_b32 %0, %3, 0" : "=s"(r) : "s"(x), "n"(M), "s"(a) : "scc");
    return r;
}
template <u32 A> DI u32 sxor(u32 f) { u32 r; asm volatile("s_xor_b32 %0, %1, %2" : "=s"(r) : "s"(f), "n"(A) : "scc"); return r; }
template <u32 A> DI u32 sadd(u32 f) { u32 r; asm volatile("s_add_u32 %0, %1, %2" : "=s"(r) : "s"(f), "n"(A) : "scc"); return r; }
#define DMA16(name, OP, C) \
template <u32 I, bool NT, class A> DI u32 name(u32 lb, u32 voff, u32x4 srd, u32 f, A a) { \
    u32 t; \
    if constexpr (NT) asm volatile("s_add_u32 m0, %1, %6\n\t" OP " %0, %4, %5\n\tbuffer_load_dwordx4 %2, %3, %0 offen nt lds" \
                                   : "=&s"(t) : "s"(lb), "v"(voff), "s"(srd), "s"(f), C((u32)a), "n"(I) : "memory", "scc"); \
    else asm volatile("s_add_u32 m0, %1, %6\n\t" OP " %0, %4, %5\n\tbuffer_load_dwordx4 %2, %3, %0 offen lds" \
                      : "=&s"(t) : "s"(lb), "v"(voff), "s"(srd), "s"(f), C((u32)a), "n"(I) : "memory", "scc"); \
    return t; \
}
DMA16(dma_xor, "s_xor_b32", "n")
DMA16(dma_add, "s_add_u32", "n")
DMA16(dma_mul, "s_mul_i32", "n")
DMA16(dma_muls, "s_mul_i32", "s")
template <bool NT> DI void dma_next(u32 voff, u32x4 srd, u32 soff) {
    if constexpr (NT) asm volatile("buffer_load_dwordx4 %0, %1, %2 offen offset:1024 nt lds" :: "v"(voff), "s"(srd), "s"(soff) : "memory");
    else asm volatile("buffer_load_dwordx4 %0, %1, %2 offen offset:1024 lds" :: "v"(voff), "s"(srd), "s"(soff) : "memory");
}
DI void dma4(u32 lds, u32 voff, u32x4 srd, u32 soff) {
    asm volatile("s_mov_b32 m0, %0\n\ts_nop 0\n\tbuffer_load_dword %1, %2, %3 offen lds" :: "s"(lds), "v"(voff), "s"(srd), "s"(soff) : "memory");
}
template <u32 I = 0> DI void ld16(u32x4& d, u32 voff, u32x4 srd, u32 soff) {
    if constexpr (NT2) asm volatile("buffer_load_dwordx4 %0, %1, %2, %3 offen offset:%4 nt" : "=v"(d) : "v"(voff), "s"(srd), "s"(soff), "n"(I) : "memory");
    else asm volatile("buffer_load_dwordx4 %0, %1, %2, %3 offen offset:%4" : "=v"(d) : "v"(voff), "s"(srd), "s"(soff), "n"(I) : "memory");
}
template <u32 I = 0> DI void ld16c(u32x4& d, u32 voff, u32x4 srd, u32 soff) {
    asm volatile("buffer_load_dwordx4 %0, %1, %2, %3 offen offset:%4 sc1" : "=v"(d) : "v"(voff), "s"(srd), "s"(soff), "n"(I) : "memory");
}
DI void ld16u(u32x4& d, u32 voff, u32x4 srd, u32 soff) {
    asm volatile("buffer_load_dwordx4 %0, %1, %2, %3 offen sc1" : "+v"(d) : "v"(voff), "s"(srd), "s"(soff) : "memory");
}
DI void ld8c(u64& d, u32 voff, u32x4 srd, u32 soff) {
    asm volatile("buffer_load_dwordx2 %0, %1, %2, %3 offen sc1" : "=v"(d) : "v"(voff), "s"(srd), "s"(soff) : "memory");
}
template <u32 I = 0> DI void st16c(u32x4 v, u32 voff, u32x4 srd) {
    asm volatile("buffer_store_dwordx4 %0, %1, %2, 0 offen offset:%3 sc1\n\ts_nop 1" :: "v"(v), "v"(voff), "s"(srd), "n"(I) : "memory");
}
template <u32 I> DI void st16c(u32x4 v, u32 voff, u32x4 srd, u32 soff) {
    asm volatile("buffer_store_dwordx4 %0, %1, %2, %3 offen offset:%4 sc1\n\ts_nop 1" :: "v"(v), "v"(voff), "s"(srd), "s"(soff), "n"(I) : "memory");
}
#define BARRIER() asm volatile("s_waitcnt lgkmcnt(0)\n\ts_barrier" ::: "memory")
template <int N> DI void waitv_barrier() { static_assert(N >= 0 && N < 64, ""); asm volatile("s_waitcnt vmcnt(%0) lgkmcnt(0)\n\ts_barrier" :: "n"(N) : "memory"); }
template <int N, class V> DI void waitv(V& r) { static_assert(N >= 0 && N < 64, ""); asm volatile("s_waitcnt vmcnt(%1)" : "+v"(r) : "n"(N) : "memory"); }
template <int N, class V> DI void waitv(V& r, V& s) { static_assert(N >= 0 && N < 64, ""); asm volatile("s_waitcnt vmcnt(%2)" : "+v"(r), "+v"(s) : "n"(N) : "memory"); }
DI void waits(u64& a, u32x16& b, u32x4& c, u32x8& d) { asm volatile("s_waitcnt lgkmcnt(0)" : "+s"(a), "+s"(b), "+s"(c), "+s"(d)); }
template <class V> DI void UNDEF(V& r) { asm volatile("" : "=v"(r)); }
template <class V> DI void UNDEFS(V& r) { asm volatile("" : "=s"(r)); }
template <class V> DI void PIN(V& r) { asm volatile("" : "+v"(r)); }
template <class V> DI void PINS(V& r) { asm volatile("" : "+s"(r)); }
template <int P> DI void prio() { if (PRIO) asm volatile("s_setprio %0" :: "n"(P)); }
template <int K> DI void flag(u32 w, u64 tag, u32x4 srd, u32 soff) {
    u64 t;
    asm volatile("s_cmp_lg_u32 %1, %5\n\ts_cbranch_scc1 .Lf%=\n\ts_mov_b64 exec, 1\n\tv_mov_b64 %0, %2\n\t"
                 "buffer_store_dwordx2 %0, off, %3, %4 sc1\n\ts_mov_b64 exec, -1\n.Lf%=:"
                 : "=&v"(t) : "s"(w), "s"(tag), "s"(srd), "s"(soff), "n"(K) : "memory", "scc");
}
#define POLL_LOOP(o) "v_cmp_ne_u64 vcc, %[tag], %[f]\n\ts_cbranch_vccz .Ld%=\n\ts_mov_b32 s99, 0x40000\n.Lp%=:\n\ts_sleep 1\n\t" \
    "buffer_load_dwordx2 %[f], %[off], %[srd], %[so] offen offset:" #o " sc1\n\ts_sub_u32 s99, s99, 1\n\ts_waitcnt vmcnt(0)\n\ts_cbranch_scc1 .Le%=\n\t" \
    "v_cmp_ne_u64 vcc, %[tag], %[f]\n\ts_cbranch_vccnz .Lp%=\n.Le%=:\n\t"
template <int N> DI void poll(u64 f, u32 off, u32x4 srd, u32 so, u64 tag) {
    static_assert(N >= 0 && N < 64, "");
    asm volatile("s_waitcnt vmcnt(%[n])\n\t" POLL_LOOP(0) "\n.Ld%=:"
                 : [f] "+v"(f) : [off] "v"(off), [srd] "s"(srd), [so] "s"(so), [tag] "s"(tag), [n] "n"(N) : "memory", "scc", "vcc", "s99");
}
DI void ld8c_w0(u64& f, u32 w, u32 off, u32x4 srd, u32 so) {
    asm volatile("s_cmp_lg_u32 %1, 0\n\ts_cbranch_scc1 .Ld%=\n\tbuffer_load_dwordx2 %0, %2, %3, %4 offen sc1\n.Ld%=:"
                 : "=v"(f) : "s"(w), "v"(off), "s"(srd), "s"(so) : "memory", "scc");
}
template <int N> DI void poll_w0(u64 f, u32 w, u32 off, u32x4 srd, u32 so, u64 tag) {
    static_assert(N >= 0 && N < 64, "");
    asm volatile("s_cmp_lg_u32 %[w], 0\n\ts_cbranch_scc1 .Ld%=\n\ts_waitcnt vmcnt(%[n])\n\t" POLL_LOOP(0) "\n.Ld%=:"
                 : [f] "+v"(f) : [w] "s"(w), [off] "v"(off), [srd] "s"(srd), [so] "s"(so), [tag] "s"(tag), [n] "n"(N) : "memory", "scc", "vcc", "s99");
}

DI u32 pk2(float a, float b) {
    f32x2 f = {a, b};
    return __builtin_bit_cast(u32, __builtin_convertvector(f, bf16x2));
}
DI bf16x8 asfrag(u32x4 v) { return __builtin_bit_cast(bf16x8, v); }
DI bf16x16 cat(bf16x8 a, bf16x8 b) { return __builtin_shufflevector(a, b, 0, 1, 2, 3, 4, 5, 6, 7, 8, 9, 10, 11, 12, 13, 14, 15); }
#define MFMA32(a, b, c) __builtin_amdgcn_mfma_f32_32x32x16_bf16((a), (b), (c), 0, 0, 0)
#define MFMA16(a, b, c) __builtin_amdgcn_mfma_f32_16x16x32_bf16((a), (b), (c), 0, 0, 0)
#define MFMA4(a, c) __builtin_amdgcn_mfma_f32_4x4x4bf16_1k((a), (a), (c), 0, 0, 0)
#define SMFMA(a, b, c, i) __builtin_amdgcn_smfmac_f32_16x16x64_bf16((a), (b), (c), (i), 0, 0)

struct Lds {
    LAS char* p;
    DI u32x4 r128(u32 a) const { return *(LAS u32x4*)(p + a); }
    DI f32x4 r128f(u32 a) const { return *(LAS f32x4*)(p + a); }
    DI void w128(u32 a, u32x4 v) const { *(LAS u32x4*)(p + a) = v; }
    DI void w128f(u32 a, f32x4 v) const { *(LAS f32x4*)(p + a) = v; }
    DI void w64(u32 a, u32x2 v) const { *(LAS u32x2*)(p + a) = v; }
    DI u32x2 r64(u32 a) const { return *(LAS u32x2*)(p + a); }
    DI void w32(u32 a, u32 v) const { *(LAS u32*)(p + a) = v; }
    DI void w32f(u32 a, float v) const { *(LAS float*)(p + a) = v; }
    DI float r32f(u32 a) const { return *(LAS float*)(p + a); }
    DI u32 r16(u32 a) const { return *(LAS unsigned short*)(p + a); }
    DI s16x4 trh(u32 a) const { return __builtin_amdgcn_ds_read_tr16_b64_v4i16((LAS s16x4*)(p + a)); }
    static DI bf16x8 join(s16x4 lo, s16x4 hi) { return __builtin_bit_cast(bf16x8, __builtin_shufflevector(lo, hi, 0, 1, 2, 3, 4, 5, 6, 7)); }
    DI bf16x8 tr(u32 a, u32 d) const { return join(trh(a), trh(a + d)); }
};

struct DualExp {
    f32x2 c, bx, bn;
    DI DualExp(const f32x16& p, float sc) {
        float mx = p[0], mn = p[0];
#pragma unroll
        for (int i = 1; i < 16; ++i) { mx = __builtin_elementwise_maximum(mx, p[i]); mn = __builtin_elementwise_minimum(mn, p[i]); }
        const float k = sc * LOG2E, x = -mx * k, n = mn * k;
        c = f32x2{k, k}; bx = f32x2{x, x}; bn = f32x2{n, n};
    }
    DI f32x2 get(const f32x16& p, int i, bool neg) const {
        const f32x2 v = {p[2 * i], p[2 * i + 1]};
        const f32x2 a = neg ? __builtin_elementwise_fma(v, -c, bn) : __builtin_elementwise_fma(v, c, bx);
        return f32x2{__builtin_amdgcn_exp2f(a[0]), __builtin_amdgcn_exp2f(a[1])};
    }
};
DI u32x4 pack8(const f32x16& a, int s) {
    return u32x4{pk2(a[8 * s], a[8 * s + 1]), pk2(a[8 * s + 2], a[8 * s + 3]), pk2(a[8 * s + 4], a[8 * s + 5]), pk2(a[8 * s + 6], a[8 * s + 7])};
}
DI f32x4 unpack4(u32 a, u32 b) { return f32x4{__uint_as_float(a << 16), __uint_as_float(a & 0xffff0000u), __uint_as_float(b << 16), __uint_as_float(b & 0xffff0000u)}; }
DI float sum4(f32x4 cp) {
    float a = cp[0] + cp[1]; PIN(a);
    a += cp[2]; PIN(a);
    a += cp[3]; PIN(a);
    return a;
}
DI bf16x8 fix_operand(float c0, u32 bias, u32 hmask) {
    const u32 chh = pk2(c0, c0);
    const u32 cl = pk2(c0 - __uint_as_float(chh << 16), 0.f);
    u32x4 wf = {chh & hmask, (cl | (bias << 16)) & hmask, bias & hmask, 0u};
    PIN(wf);
    return asfrag(wf);
}
DI float add32(float a, float b) {
    const auto s = __builtin_amdgcn_permlane32_swap(__float_as_uint(a), __float_as_uint(b), false, false);
    return __uint_as_float(s[0]) + __uint_as_float(s[1]);
}

#define STAMPOP "s_memrealtime"
#define SSET 0
#define STAMPX(set, i) do {} while (0)
#define STAMP(i) STAMPX(0, i)

constexpr int ustep(int u) { return u < 32 ? u >> 2 : (u < 38 ? 8 + (u - 32) % 3 : 8 + (u - 38) / 2); }
constexpr int ucol(int u) { return u < 32 ? u & 3 : (u < 38 ? 2 + (u - 32) / 3 : (u - 38) & 1); }
constexpr int lcell(int id) { return id < 20 ? id : (id == 20 ? 19 : (id < 23 ? id - 13 : id - 23)); }

#define KAL 4096
__global__ void __launch_bounds__(512, 2) __attribute__((aligned(KAL)))
#define A2(n) const void* p##n, u64 a##n
#define A4(n) A2(n), u64 b##n, u64 c##n
hedgehog_mixer_kernel(A2(0), A2(1), A2(2), A2(3), A2(4), A2(5), A2(6), A2(7), A4(8), A4(9), A4(10), A4(11), A4(12), A4(13)) {
#if __HIP_DEVICE_COMPILE__
    __shared__ __attribute__((aligned(16))) char smem[163840];
    const Lds L{(LAS char*)smem};
    u32 b_ = blockIdx.x, l_ = threadIdx.x & 63, w_;
    u32x8 ka, rec; u32x16 kb;
    u32x4 srdx, srdw, srdw3, selu;
    const u64 kp = (u64)__builtin_amdgcn_kernarg_segment_ptr();
    asm volatile("s_load_dwordx8 %0, %2, 0\n\ts_load_dwordx4 %1, %2, %3" : "=&s"(ka), "=&s"(srdw) : "s"(kp), "s"(32 + ((b_ >> 2) & 16)));
    w_ = __builtin_amdgcn_readfirstlane(threadIdx.x >> 6);
    u32 vx0, vx1, vw0, vw1, w2k, k0x, kx, kw, xa[4], wa[4], sidx;
    u64 epoch;
    auto tile = [&](auto A, auto wh) {
        constexpr int a = A;
        constexpr u32 s = ((a >> 2) & 3) * 16384;
        constexpr bool ntx = (NTX >> (a >> 2)) & 1, ntw = (NTW >> (a >> 2)) & 1;
        if constexpr ((a & 3) == 0) kx = dma_xor<s, ntx>(w2k, vx0, srdx, k0x, IC<(a >> 2) * 128>{});
        else if constexpr ((a & 3) == 1) dma_next<ntx>(vx1, srdx, kx);
        else if constexpr (a == 2 || a == 6) kw = dma_muls<L_W + s, ntw>(w2k, vw0, srdw, kx, wh);
        else if constexpr ((a & 3) == 2) kw = dma_mul<L_W + s, ntw>(w2k, vw0, srdw, kx, wh);
        else dma_next<ntw>(vw1, srdw, kw);
    };
    {
        const u32 b = b_, l = l_, w = w_, xcd = b & 7, bi = b >> 3;
        {
            u64 pc; u32 len;
            asm volatile("s_getpc_b64 %0\n.Lpf_here:\n\ts_mov_b32 %1, .Lpf_end-.Lpf_here" : "=s"(pc), "=s"(len));
            const u32x4 sc = mk_srd((u32)pc, (u32)(pc >> 32), len);
            asm volatile("s_mov_b32 m0, %0\n\ts_nop 3\n\tbuffer_load_dword off, %1, %2 lds" :: "n"(L_DUMP), "s"(sc), "s"((w * 32 + bi) * 128) : "memory");
        }
        const u32 rt = RHALF * 8 + (bi & 7), isx = (bi >> 3) & 1, ci = CGRP * 2 + (bi >> 4);
        const u32 k0 = 4 * ((rt ^ isx) & 1);
        const u32 wrow = isx ? 3072 : 2752, l3 = l >> 3;
        vx0 = (rt * 128 + 16 * w + l3) * 1024 + 16 * ((l & 7) ^ (l >> 4)); vx1 = (vx0 ^ 64) + 7168;
        vw0 = (8 * w + (l3 & 3)) * wrow + (isx ? 1024 + ci * 128 : ci * 256) + (l >> 5) * (isx ? 1024 : 128) + 16 * ((l & 7) ^ (4 * ((l >> 4) & 1)));
        vw1 = vw0 + 4 * wrow - 1024;
        w2k = w * 2048; k0x = k0 * 128;
        u32 wh = wrow / 2;
        PINS(w2k); PINS(k0x); PINS(wh);
        PIN(vx0); PIN(vx1); PIN(vw0); PIN(vw1);
        asm volatile("s_waitcnt lgkmcnt(0)" : "+s"(ka), "+s"(srdw));
        STAMPX(2, 1);
        srdx = __builtin_shufflevector(ka, ka, 0, 1, 2, 3);
        rep<0, 4>([&](auto A) { tile(A, wh); });
        asm volatile("s_load_dwordx2 %0, %4, 0\n\ts_load_dwordx16 %1, %5, 64\n\ts_load_dwordx4 %2, %5, %6\n\ts_load_dwordx8 %3, %5, %7"
                     : "=&s"(epoch), "=&s"(kb), "=&s"(srdw3), "=&s"(rec)
                     : "s"((u64)ka[4] | ((u64)ka[5] << 32)), "s"(kp), "s"(48 - 16 * isx), "s"(128 + 32 * min(w, 5u)));
        SB();
        PIN(l_);
        const u32 m = l_, h = m >> 5, tg = w & 3, hp = w >> 2, rot = tg ^ (2 * hp), hm = (m >> 2) & 1;
        xa[0] = (32 * tg + (m & 31)) * 128 + 16 * ((2 * rot + h) ^ ((m >> 1) & 7));
        wa[0] = L_W + (4 * rot + 2 * h) * 1024 + (m & 12) * 32 + 64 * (hp ^ ((m >> 3) & 1)) + (m & 16) * 2 + (m & 1) * 16 + (m & 2) * 4;
        PIN(xa[0]); PIN(wa[0]);
#pragma unroll
        for (int i = 1; i < 4; ++i) { xa[i] = xa[0] ^ (32 * i); wa[i] = wa[0] ^ (4096 * i); PIN(xa[i]); PIN(wa[i]); }
        const u32 one = (((m >> 3) ^ h) & 1) ? 0u : 0x3f803f80u;
        selu = u32x4{one & (hm - 1), one & (hm - 1), one & -hm, one & -hm};
        sidx = (m & 1) ? 0xeeee : 0x4444;
        PIN(selu); PIN(sidx);
        SB();
        STAMPX(2, 2);
        asm volatile("s_barrier");
        rep<4, 8>([&](auto A) { tile(A, wh); });
        STAMPX(2, 3);
    }

    auto body = [&](auto XX) {
        constexpr bool X = XX;
        constexpr bool SH = slot_of<true>(CLR, 1) != -99, SO = slot_of<true>(CLR, 3) != -99;
        constexpr int FK = X ? (SH ? -1 : XFG) : (SO ? -1 : YFG);
        constexpr u32 wrow = X ? 3072 : 2752, wrow3 = X ? 2752 : 3072;
        constexpr int MB = X ? XMIDB : MIDB, BF = B0 ? B0 : BA;
        u32 b = b_, l = l_, w = w_;
        PINS(b); PINS(w); PIN(l);
        const u32 xcd = b & 7, bi = b >> 3, rt = RHALF * 8 + (bi & 7), ci = CGRP * 2 + (bi >> 4);
        const u32 rb = xcd * 4 + (bi & 3), cb = bi >> 2, prt = rb >> 1;
        const u32 tg = w & 3, hp = w >> 2, kq = 2 * (tg & 1) + hp;
        const u32x4 srdws = __builtin_shufflevector(ka, ka, 4, 5, 6, 7);
        u32x4 srd2, srdo, ssrd;
        u64 tag;
        u32 l16, w1k, vw3, vw4, v2, f32k, tb2, pw0, pw1, hmask, csa, b1a, l4, sta;
        u32 hso, ob, mv, mso, msz, swo, swl, fls, ko, va, pa, a0, b0, ho;
        u32x4 msrd;
        l16 = l * 16; l4 = l * 4; w1k = w * 1024;
        PIN(l16); PIN(l4); PINS(w1k);
        auto setup = [&](int part) {
            PIN(l);
            const u32 r = l & 31, h = l >> 5, l3 = l >> 3, q = (l & 15) >> 2, p = l & 3, blk = (l >> 4) & 1, g = l >> 4, tk = l & 15, hm = (l >> 2) & 1;
            if (part == 0) {
                waits(epoch, kb, srdw3, rec);
                srd2 = __builtin_shufflevector(kb, kb, 0, 1, 2, 3); srdo = __builtin_shufflevector(kb, kb, 4, 5, 6, 7); ssrd = __builtin_shufflevector(kb, kb, 8, 9, 10, 11);
                tag = epoch + 1;
                mso = rec[4] + rec[5] * seq<0>(hp, b, seq<4>(w, X ? 16 + ci : 2 * ci, cb));
                msz = min(rec[2], mso + (rec[6] >> 16));
                msrd = u32x4{rec[0], rec[1], 0u, 0x00020000u};
                mv = l3 * (rec[6] & 0xffff) + (l & 7) * rec[7];
                swo = ci * 2048 + w * 256; swl = L_SW + w * 256;
                PINS(msrd); PINS(msz); PINS(mso); PINS(swo); PINS(swl); PIN(mv);
            } else if (part == 1) {
                const u32 c3 = (X ? 2048 : 0) + ci * 128 + 16 * ((l & 7) ^ (4 * blk));
                vw3 = (8 * w + (l3 & 3) + 64 * (h ^ ((k0x >> 7) & 1))) * wrow3 + c3;
                PIN(vw3);
                if (X) vw3 = c3 < wrow3 ? vw3 : OOB;
                vw4 = vw3 + 4 * wrow3 - 1024;
                PIN(vw3); PIN(vw4);
            } else if (part == 2) {
                v2 = (256 * hp + 8 * tg + l3) * 1024 + cb * 128 + 16 * ((l & 7) ^ (2 * (blk | ((w & 1) << 1))));
                f32k = (cb ^ (2 * (bi & 3))) * 32768;
                tb2 = (32 - 5 * hp) * 32768;
                PIN(v2); PINS(f32k); PINS(tb2);
            } else if (part == 3) {
                hmask = h - 1; csa = L_CS + (hp * 32 + r) * 16; sta = 18 * 8192 + w1k + l4;
                b1a = L_B1 + (hp * 32 + (l & 16) + 4 * ((hm << 1) | (l3 & 1)) + p) * 2;
                PIN(hmask); PIN(csa); PIN(b1a); PIN(sta);
            } else if (part == 4) {
                const u32 pw = L_SW + (2 * hp + (p & 1)) * 512 + (8 * h + q) * 32 + (2 * blk + (p >> 1)) * 8;
                pw0 = (p & 1) ? L_ZERO : pw; pw1 = (p & 1) ? pw : L_ZERO;
                PIN(pw0); PIN(pw1);
            } else if (part == 5) {
                if (w == 6) { const u32 v = (l >> 1) == 1 ? 0x3f803f80u : 0u; L.w128(L_ZERO + l16, u32x4{v, v, v, v}); }
                if (!X && w == 7) {
                    const u64 v = (u64)((l3 ^ g) & 1 ? 0u : 0x3f80u) << (16 * p);
                    L.w128(L_ONE + l16, u32x4{(u32)v & (hm - 1), (u32)(v >> 32) & (hm - 1), (u32)v & -hm, (u32)(v >> 32) & -hm});
                }
            } else if (part == 6 && X) {
                u32 kz = tk ? OOB : l, kk = h * 1024 + r * 16;
                PIN(kz); PIN(kk);
                ko = (hp ? kz : kk) + seq<0>(hp, WS_PKV + ci * 65536 + (rt & 7) * 8192 + tg * 2048 + (rt >> 3) * 512, WS_PZ + (ci * 16 + rt) * 256 + tg * 64);
                hso = 2 * ci + hp < 11 ? WS_H + rt * HRT + (32 + 2 * ci) * 8192 + w2k : OOB; ob = WS_O + rt * 131072 + ci * 16384 + w2k;
                PIN(ko); PINS(hso); PINS(ob);
            } else if (part == 6) {
                ho = WS_H + rt * HRT + (4 * ci + hp) * 8192 + (2 * tg + blk) * 1024 + h * 256 + tk * 16;
                PIN(ho);
            } else if (part == 7) {
                va = L_V + (32 * tg + r) * 128 + 16 * ((4 * hp + h) ^ (r & 7));
                pa = L_PHI + ((2 * hp + h) * 128 + 32 * tg + r) * 64 + 16 * ((r >> 1) & 3);
                PIN(va); PIN(pa);
            } else {
                a0 = L_PHI + (tg * 128 + 4 * g + q) * 64 + 16 * ((p >> 1) ^ (2 * blk + (q >> 1))) + 8 * (p & 1);
                b0 = L_V + (4 * g + q) * 128 + 16 * ((2 * tg + (p >> 1)) ^ (4 * blk + q)) + 8 * (p & 1);
                PIN(a0); PIN(b0);
            }
        };
        u32x4 raw[31], hf[22], gt[8], of[8], xr;
        u32 hv[2], flv, outo, hb, gb, zw, wl[3], ht[3];
        u32x4 unset;
        u64 fl, ef[2];
        u32 ew;
        auto op = [&](auto K, auto A, auto S) {
            constexpr int k = K, a = A, c = a < 20 ? a : a - 1;
            constexpr u32 po = a < 16 ? (a >> 1) * 32768 + (a & 1) * 0x80000 : (a < 23 ? (6 * (c & 1) + (c >> 1) - 8) * 32768 : (a - 23) * 32768);
            if constexpr ((SKIP >> k) & 1) {
                if constexpr (k == RAW) UNDEF(raw[a]); else if constexpr (k == HL) UNDEF(hf[a]); else if constexpr (k == OL) UNDEF(of[a]);
            } else if constexpr (k == T) tile(A, IC<wrow / 2>{});
            else if constexpr (k == SU) setup(a);
            else if constexpr (k == MISC) { msrd[2] = sbit<a | 0xf0>(w, msz); dma4(rec[3], mv, msrd, mso); }
            else if constexpr (k == SWL) dma4(swl, l4, ssrd, swo);
            else if constexpr (k == W3 && !(a & 1)) kw = dma_mul<L_W + a * 8192, NT3>(w2k, vw3, srdw3, sxor<a * 128>(k0x & 0x300), IC<wrow3 / 2>{});
            else if constexpr (k == W3) dma_next<NT3>(vw4, srdw3, kw);
            else if constexpr (k == CELL || k == LD) {
                if constexpr (a >= 16 && a < 23) dma_add<lcell(a) * 8192, NT2>(w1k, v2, srd2, tb2, IC<po>{});
                else dma_xor<lcell(a) * 8192, NT2>(w1k, v2, a < 16 ? srd2 : srdo, f32k, IC<po>{});
            } else if constexpr (k == RAW) ld16(raw[a], v2, a < 23 ? srd2 : srdo, a >= 16 && a < 23 ? sadd<po>(tb2) : sxor<po>(f32k));
            else if constexpr (k == LW) { waitv<after<X>(RAW, a, S)>(raw[a]); L.w128(wl[lcell(a) >> 3] + (a == 20 ? 0 : (lcell(a) & 7) * 8192), raw[a]); }
            else if constexpr (k == GA) ld16c(gt[a], l16, srdws, sxor<a * 8192>(gb));
            else if constexpr (k == ZW) {
                gb = WS_PKV + ci * 65536 + (rt & 7) * 8192 + w1k;
                PINS(gb);
                if (w == ZWV) {
                    rep<0, 4>([&](auto M) { ld16c<M * 1024>(of[M], l16, srdws, WS_PZ + ci * 4096); });
                    zw = L_ZERO + 128 + (l & 12) * 64 + (l & 3) * 16 + ((l >> 1) & 8) + ((l >> 3) & 4);
                    PIN(zw);
                }
            } else if constexpr (k == HL) {
                if constexpr (a < 16 && !(a & 1)) hso = sxor<(a >> 1) * 8192>(hb);
                else if constexpr (a == 16) { ht[0] = (hb & ~0xe000u) + (32 - 5 * kq) * 8192; PINS(ht[0]); }
                else if constexpr (a == 18) ht[1] = sadd<8192>(ht[0]);
                else if constexpr (a == 20) { ht[2] = seq<3>(kq, OOB, ht[1] + 8192); PINS(ht[2]); }
                ld16c(hf[a], hv[a & 1], srdws, a < 16 ? hso : ht[a < 16 ? 0 : (a >> 1) - 8]);
            } else if constexpr (k == FW || k == FL) {
                fls = a == 0 ? WS_FP + ci * 1024 : WS_FH * a + prt * 1024;
                if constexpr (a < 2) { flv = l16 & (a == 0 ? 0x3c0 : 0x380); PIN(flv); }
                PINS(fls);
                if constexpr (k == FW) ld8c_w0(fl, w, flv, srdws, fls); else ld8c(fl, flv, srdws, fls);
            } else if constexpr (k == PL) poll<after<X>(FL, a, S)>(fl, flv, srdws, fls, tag);
            else if constexpr (k == EL) {
                ew = (b * 8 + w) ^ EPW; ko = l * 128;
                PINS(ew); PIN(ko);
                ld8c_w0(ef[0], ew, ko, srdws, WS_FH); ld8c_w0(ef[1], ew, ko, srdws, WS_FH + 8192);
            } else if constexpr (k == EP) {
                poll_w0<after<X>(EL, 0, S)>(ef[0], ew, ko, srdws, WS_FH, tag); poll_w0<after<X>(EL, 0, S)>(ef[1], ew, ko, srdws, WS_FH + 8192, tag);
                flag<0>(ew, tag, srdws, 0);
            } else if constexpr (k == XR) ld16(xr, outo, srdx, 0);
            else if constexpr (k == OL) ld16c(of[a], hv[0], srdws, sxor<a * 8192>(ob));
        };
        auto ops = [&](auto S) {
            constexpr int s = S;
            rep<0, -1 - nth<X>(s, -1)>([&](auto I) { constexpr Op o = opat<X>(nth<X>(s, I)); op(IC<o.kind>{}, IC<o.arg>{}, S); });
            rep<0, 5>([&](auto A) {
                if constexpr (slot_of<X>(CLR, A) == s) {
                    if constexpr (A == 0) { unset = u32x4{~0u, ~0u, ~0u, ~0u}; PIN(unset); st16c(unset, ko, srdws); }
                    else st16c<(A & 1) * 1024>(unset, l16, srdws, A < 3 ? hso : ob);
                }
            });
        };
        auto settle = [&](auto n, u32x4* d, auto load) {
            auto unset_in = [&] { u32 m = 0; rep<0, n>([&](auto J) { m = max(m, d[J][0]); }); return __builtin_amdgcn_ballot_w64(m == ~0u) != 0; };
            if (__builtin_expect(unset_in(), 0)) {
                int spin = 1 << 17;
                do {
                    __builtin_amdgcn_s_sleep(2);
                    rep<0, n>([&](auto J) { if (__builtin_amdgcn_ballot_w64(d[J][0] == ~0u) != 0) load(J); });
                    rep<0, n>([&](auto J) { waitv<0>(d[J]); });
                } while (unset_in() && --spin);
            }
        };
        auto pollw = [&](auto A, auto S) { if constexpr (slot_of<X>(FW, A) != -99) poll_w0<after<X>(FW, A, S)>(fl, w, flv, srdws, fls, tag); };
        STAMPX(2, 4);
        ops(IC<-1>{});
        STAMP(1); STAMPX(2, 5);

        u32x4 xf[32];
        f32x16 acc0 = {}, acc1 = {};
        float rs1, ga, gb2, sdv;
        bf16x8 xfix, wp0, wp1;
        u32 bias0, bias1;
        waitv_barrier<after<X>(T, 3, 0)>();
        STAMP(2); STAMPX(2, 6); STAMPX(1, 2);
        {
            bf16x8 wc[32][2], wf0, wf1;
            f32x4 cs = {}, cp0, cp1, sq[2] = {}, sm = {};
            float mean, var, v2n, st, pv;
            auto rdx = [&](int m) { xf[m] = L.r128(xa[m & 3] + ((m >> 2) & 3) * 16384); };
            auto rdw = [&](int m, int j) { wc[m][j] = L.tr(wa[m & 3] + ((m >> 2) & 3) * 16384 + 512 * j, 1024); };
            auto sums = [&](int c, int m) {
                SB();
                cs = SMFMA(asfrag(selu), cat(wc[c][0], wc[c][1]), cs, sidx);
                sm = SMFMA(asfrag(selu), cat(asfrag(xf[m]), asfrag(xf[m + 1])), sm, sidx);
                PIN(cs); PIN(sm);
#pragma unroll
                for (int j = 0; j < 4; ++j) {
                    SB();
                    sq[j & 1] = MFMA4(__builtin_bit_cast(s16x4, u32x2{xf[m + (j >> 1)][2 * (j & 1)], xf[m + (j >> 1)][2 * (j & 1) + 1]}), sq[j & 1]);
                    PIN(sq[j & 1]);
                }
                PIN(cs); PIN(sm); PIN(selu);
            };
            rdx(0); rdw(0, 0); rdw(0, 1); rdx(1); rdw(1, 0); rdw(1, 1);
            ops(IC<0>{});
            rep<0, 32>([&](auto N) {
                constexpr int n = N, t = n >> 2, i = n & 3;
                if constexpr (n < 28 ? i == 2 : n == 29) {
                    STAMPX(1, 3 + t);
                    waitv_barrier<cmin(after<X>(T, 4 * t + 7, 1 + 2 * n), cmin(n == FPAT ? after<X>(CLR, 0, 4, 1 + 2 * n) : 63, n == 26 ? after<X>(SWL, 0, 1 + 2 * n) : 63))>();
                }
                SB();
                acc0 = MFMA32(wc[n][0], asfrag(xf[n]), acc0);
                PIN(acc0);
                SB();
                prio<n < 26 ? 3 - ((n + 2) & 3) : (n < 29 ? 28 - n : 3)>();
                if constexpr (n < 26) { rdx(n + 2); rdw(n + 2, 0); if (i == 1) rdw(n + 2, 1); }
                if constexpr (n == 26) { rdw(28, 0); rdw(28, 1); rdx(28); }
                if constexpr (n == 27) { rdw(29, 1); rdx(30); rdw(30, 0); }
                if constexpr (n == 28) { rdx(31); rdw(31, 0); rdw(31, 1); }
                if constexpr (n == 29) { pv = L.r32f(sta); cp0 = L.r128f(csa + 1024); cp1 = L.r128f(csa + 2048); bias0 = L.r16(b1a); if (!X) bias1 = L.r16(b1a + 128); }
                ops(IC<1 + 2 * n>{});
                if constexpr (X && n == FPAT) flag<1>(w, tag, srdws, WS_FP + (ci * 16 + rt) * 64);
                if constexpr (n == 30) {
                    const u32 hs = pk2(-mean, sdv), ls = pk2(-mean - __uint_as_float(hs << 16), sdv - __uint_as_float(hs & 0xffff0000u));
                    u32x4 xu = {(hs & 0xffffu) | (ls << 16), hs, ls >> 16, 0u};
                    PIN(xu);
                    xfix = asfrag(xu);
                }
                if constexpr (n == 31) wf1 = fix_operand(sum4(cp1), X ? 0 : bias1, hmask);
                SB();
                acc1 = MFMA32(wc[n][1], asfrag(xf[n]), acc1);
                PIN(acc1);
                SB();
                if constexpr (n < 26 && i != 1) rdw(n + 2, 1);
                if constexpr (n == 26) { rdx(29); rdw(29, 0); }
                if constexpr (n == 27) rdw(30, 1);
                ops(IC<2 + 2 * n>{});
                if constexpr (i == SUMI && t < 7) sums(n, 4 * t);
                if constexpr (n == 27) sums(28, 28);
                if constexpr (n == 28) {
                    L.w32f(csa + 1024 + (~hmask & 1024) + tg * 4, cs[0] + cs[1]);
                    float s[4];
#pragma unroll
                    for (int j = 0; j < 4; ++j) { s[j] = sq[0][j] + sq[1][j]; PIN(s[j]); }
                    const float d0 = (l & 1) ? s[1] : s[0], d1 = (l & 1) ? s[3] : s[2];
                    st = add32(sm[0] + sm[1], (l & 2) ? d1 : d0);
                    L.w32f(sta ^ 4096, st);
                }
                if constexpr (n == 29) {
                    st += pv;
                    const auto sf = __builtin_amdgcn_permlane32_swap(__float_as_uint(st), __float_as_uint(st), false, false);
                    mean = __uint_as_float(sf[0]) * (1.f / 512.f);
                    PIN(mean);
                    var = fmaxf(fmaf(__uint_as_float(sf[1]), 1.f / 512.f, -mean * mean), 0.f);
                    v2n = fmaf(var + 1e-5f, 1e-5f, var);
                    sdv = __builtin_amdgcn_sqrtf(v2n);
                    PIN(sdv); PIN(var); PIN(mean); PIN(v2n);
                }
                if constexpr (n == 30) wf0 = fix_operand(sum4(cp0), X ? 0 : bias0, hmask);
                if constexpr (n == 31) {
                    const float rs2 = __builtin_amdgcn_rsqf(v2n);
                    rs1 = __builtin_amdgcn_rsqf(var + 1e-5f);
                    ga = -2.f * 0.7978845608028654f * LOG2E * rs2; gb2 = ga * 0.044715f * rs2 * rs2;
                    PIN(rs1); PIN(ga); PIN(gb2);
                }
                if constexpr (X && n == 31) { wp0 = L.tr(pw0, 128); wp1 = L.tr(pw1, 128); }
                SB();
            });
            acc0 = MFMA32(wf0, xfix, acc0);
            PIN(acc0);
            SB();
            acc1 = MFMA32(wf1, xfix, acc1);
            PIN(acc1);
            SB();
            prio<3>();
        }
        STAMP(3); STAMPX(1, 11); STAMPX(3, 1);
        auto logits = [&](const f32x16& a) { return MFMA32(wp1, asfrag(pack8(a, 1)), MFMA32(wp0, asfrag(pack8(a, 0)), f32x16{})); };
        u32 ba[4];
        u32x2 b2r[2];
        f32x4 am[2][4];
        auto phase_b_setup = [&](int part) {
            if (part == 0) {
                hv[0] = l16 + (tg & 1) * 1024; hv[1] = hv[0] ^ 1024;
                hb = WS_H + prt * HRT + kq * 65536 + (f32k >> 2) + (4 * (rb & 1) + (tg & 2)) * 1024;
                PIN(hv[0]); PIN(hv[1]); PINS(hb);
            } else if (part == 1) {
                ba[0] = (l & 48) * 64 + (l & 12) * 32 + (l & 3) * 8;
                PIN(ba[0]);
            } else if (part == 2) {
                ba[0] += ((l & 24) * 4) ^ (kq * 4096 + 64 * hp);
                PIN(ba[0]);
                const u32 a = L_B2 + 64 * hp + ((l >> 1) & 24);
                b2r[0] = L.r64(a); b2r[1] = L.r64(a + 32);
            } else if (part == 3) {
#pragma unroll
                for (int c = 1; c < 4; ++c) { ba[c] = ba[0] ^ (32 * c); PIN(ba[c]); }
            } else if (part < 6) {
                am[0][part - 4] = unpack4(b2r[part - 4][0], b2r[part - 4][1]);
                PIN(am[0][part - 4]);
            } else if (part == 6) {
                wl[0] = w1k + l16; wl[1] = wl[0] + 65536; wl[2] = wl[0] + L_SCR;
                PIN(wl[0]); PIN(wl[1]); PIN(wl[2]);
            }
        };
        auto pass2 = [&](auto step, auto sync) {
            bf16x8 f[3], c0, c1, wf;
            f32x16 acc = {};
            f32x4 cs = {}, cp;
            float cs0;
            PIN(l);
            const u32 hm = (l >> 2) & 1;
            u32x4 sel2 = {0x3f803f80u & (hm - 1), 0x3f803f80u & (hm - 1), 0x3f803f80u & -hm, 0x3f803f80u & -hm};
            PIN(sel2); PIN(cs);
            STAMPX(3, 2);
            sync(IC<0>{});
            STAMPX(3, 3);
            f[0] = L.tr(wa[0], 1024); f[1] = L.tr(wa[1], 1024);
            rep<0, 32>([&](auto N) {
                constexpr int n = N, m = n + 2;
                if constexpr (n == 5) STAMPX(3, 4);
                if constexpr (n == 10) STAMPX(3, 5);
                if constexpr (n == MB) STAMPX(3, 6);
                if constexpr (n == 20) STAMPX(3, 8);
                if constexpr (n == 25) STAMPX(3, 9);
                if constexpr (n == 30) STAMPX(3, 10);
                if constexpr (n == MB || n == 30) sync(N);
                if constexpr (n == MB) STAMPX(3, 7);
                if constexpr (n == 30) STAMPX(3, 11);
                SB();
                acc = MFMA32(f[n % 3], asfrag(xf[n]), acc);
                PIN(acc);
                SB();
                if constexpr (n == 0 || n == MB) prio<3>();
                if constexpr (n == MB - 8 || n == (X ? 22 : 26)) prio<2>();
                if constexpr (n == MB - 4 || (X && n == 26)) prio<1>();
                if constexpr (n == MB - 2 || (X && n == 28)) prio<0>();
                if constexpr (m < 32) f[m % 3] = L.tr(wa[m & 3] + 16384 * (m >> 3) + 512 * ((m >> 2) & 1), 1024);
                if constexpr (n >= 2 && n <= 8 && !(n & 1)) { cs = SMFMA(asfrag(sel2), cat(c0, c1), cs, sidx); PIN(cs); SB(); }
                if constexpr (n <= 6 && !(n & 1)) { c0 = L.tr(wa[0] + 8192 * n, 1024); c1 = L.tr(wa[0] + 8192 * n + 512, 1024); }
                if constexpr (n == 11) L.w32f(csa + tg * 4, cs[0] + cs[1]);
                if constexpr (n == MB) cp = L.r128f(csa);
                if constexpr (n == MB + 1) cs0 = sum4(cp);
                if constexpr (n == MB + 2) wf = fix_operand(cs0, X ? bias0 : 0, hmask);
                if constexpr (n == MB + 4) { acc = MFMA32(wf, xfix, acc); PIN(acc); }
                ops(IC<200 + n>{});
                step(N);
                SB();
            });
            return acc;
        };
        auto gelu2 = [&](float a, float c) {
            const f32x2 gap = {ga, ga}, gbp = {gb2, gb2}, sdp = {sdv, sdv}, v = {a, c};
            const f32x2 z = v * __builtin_elementwise_fma(v * v, gbp, gap);
            const f32x2 e = {__builtin_amdgcn_exp2f(z[0]), __builtin_amdgcn_exp2f(z[1])};
            const f32x2 d = __builtin_elementwise_fma(e, sdp, sdp);
            const f32x2 y = v * f32x2{__builtin_amdgcn_rcpf(d[0]), __builtin_amdgcn_rcpf(d[1])};
            return pk2(y[0], y[1]);
        };

        if constexpr (X) {
            {
                const f32x16 pk = logits(acc0);
                acc1 *= rs1;
                L.w128(va, pack8(acc1, 0));
                L.w128(va ^ 32, pack8(acc1, 1));
                const DualExp de(pk, rs1);
                rep<0, 2>([&](auto NG) {
                    constexpr int ng = NG;
                    f32x2 e[8], s;
                    rep<0, 4>([&](auto I) {
                        SB();
                        ops(IC<100 + 4 * ng + I>{});
                        if constexpr (ng == 1 && I == 0) prio<2>();
                        if constexpr (ng == 1 && I == 3) prio<1>();
                        e[2 * I] = de.get(pk, 2 * I, ng); e[2 * I + 1] = de.get(pk, 2 * I + 1, ng);
                        if constexpr (I == 0) s = e[0] + e[1]; else s += e[2 * I] + e[2 * I + 1];
                    });
                    const float rc = __builtin_amdgcn_rcpf(s[0] + s[1]);
#pragma unroll
                    for (int c = 0; c < 2; ++c) {
                        u32x4 o;
#pragma unroll
                        for (int j = 0; j < 4; ++j) { const f32x2 a = e[2 * c + (j & 1) + 2 * (j & 2)] * rc; o[j] = pk2(a[0], a[1]); }
                        L.w128(pa ^ (32 * c + 16 * ng), o);
                        if (ng == 1 && c == 0) { SB(); prio<0>(); }
                    }
                });
            }
            {
                u32 a1 = a0 ^ 32;
                PIN(a1);
                waitv_barrier<cmin(after<X>(W3, 7, 150), after<X>(CLR, 0, 4, 150))>();
                if constexpr (FPAT == 150) flag<7>(w, tag, srdws, WS_FP + (ci * 16 + rt) * 64);
                STAMP(4);
                bf16x8 af[4][2], vf[4];
#pragma unroll
                for (int s = 0; s < 4; ++s) { af[s][0] = L.tr(a0 + s * 2048, 1024); vf[s] = L.tr(b0 + s * 4096, 2048); af[s][1] = L.tr(a1 + s * 2048, 1024); }
                if (hp) {
#pragma unroll
                    for (int s = 0; s < 4; ++s) vf[s] = asfrag(u32x4{0x3f803f80u, 0x3f803f80u, 0x3f803f80u, 0x3f803f80u});
                }
                f32x4 k0 = {}, k1 = {};
#pragma unroll
                for (int s = 0; s < 4; ++s) { k0 = MFMA16(af[s][0], vf[s], k0); k1 = MFMA16(af[s][1], vf[s], k1); }
                st16c(u32x4{pk2(k0[0], k0[1]), pk2(k0[2], k0[3]), pk2(k1[0], k1[1]), pk2(k1[2], k1[3])}, ko, srdws);
                SB();
            }
            STAMP(5);
            const f32x16 acc2 = pass2([&](auto N) {
                constexpr int n = N;
                if constexpr (n == 9) { ho = (l & 16) * 64 + (l & 32) * 8 + (l16 & 0xf0); PIN(ho); }
                if constexpr (n == 10) { ho += hso; PIN(ho); }
                if constexpr (n >= 20 && n < 27) phase_b_setup(n - 20);
            }, [&](auto N) {
                if constexpr (N == MB) { BARRIER(); STAMP(6); }
                else if constexpr (N == 30 && slot_of<X>(FW, 1) != -99) { pollw(IC<1>{}, IC<230>{}); BARRIER(); }
            });
            STAMP(7);
            prio<3>();
            u32x4 o;
            rep<0, 8>([&](auto I) {
                ops(IC<300 + I>{});
                if constexpr (I == 4) prio<2>();
                if constexpr (I == 6) prio<1>();
                o[I & 3] = gelu2(acc2[2 * I], acc2[2 * I + 1]);
                if constexpr ((I & 3) == 3) st16c<(I >> 2) * 512>(o, ho, srdws);
                if constexpr (I & 1) SB();
            });
            ops(IC<308>{});
            waitv_barrier<cmin(after<X>(CELL, 0, 2 * BF + 1, 400), FK ? 63 : after<X>(HST, 1, 400))>();
            STAMP(8);
        } else {
            {
                u32x4 o;
                rep<0, 16>([&](auto I) {
                    constexpr int i = I;
                    ops(IC<100 + i>{});
                    if constexpr (i == 8) { prio<2>(); ho += 16384; }
                    if constexpr (i == 12) prio<1>();
                    if constexpr (i == 14) prio<0>();
                    o[i & 3] = i < 8 ? gelu2(acc0[2 * (i & 7)], acc0[2 * (i & 7) + 1]) : gelu2(acc1[2 * (i & 7)], acc1[2 * (i & 7) + 1]);
                    if constexpr ((i & 3) == 3) st16c<((i >> 2) & 1) * 512>(o, ho, srdws);
                    if constexpr (i & 1) SB();
                });
            }
            STAMP(4);
            u32 kvw, fa, dz0, dz1, os;
            u32x4 one;
            const f32x16 acc2 = pass2([&](auto N) {
                constexpr int n = N;
                if constexpr (n == 9) { os = ho + (l & 32) * 8 + (WS_O - WS_H + rt * (131072 - HRT) - (2 * ci + 2) * 8192); PIN(os); }
                if constexpr (n == 10) { kvw = (ho & 0x1f0) + ((l >> 1) & 8) + (L_KV + (w1k >> 1)); PIN(kvw); }
                if constexpr (n == 12) { wp0 = L.tr(pw0, 128); wp1 = L.tr(pw1, 128); }
                if constexpr (n == 13) { fa = (l & 32) * 32 + (l & 24) * 8 + (l16 & 48) + (L_KV + hp * 2048); PIN(fa); }
                if constexpr (n == 14) { fa = ((l >> 3) ^ l) & 4 ? L_ZERO : fa; PIN(fa); }
                if constexpr (n == 15) { dz1 = (l & 32) * 8 + (l & 1) * 32 + (L_ZERO + 128 + hp * 512); PIN(dz1); }
                if constexpr (n == 19) { dz1 = (l & 2) ? L_ZERO + 32 : dz1; PIN(dz1); }
                if constexpr (n >= 20 && n < 27) phase_b_setup(n - 20);
                if constexpr (n == 27) { dz1 = ((l >> 2) ^ l) & 12 ? L_ZERO : dz1; PIN(dz1); }
                if constexpr (n == 28) { dz0 = (l & 1) ? L_ZERO : dz1; dz1 = (l & 1) ? dz1 : L_ZERO; PIN(dz0); PIN(dz1); }
                if constexpr (n == 29) one = L.r128(L_ONE + l16);
            }, [&](auto N) {
                if constexpr (N == 0) {
                    if constexpr (slot_of<X>(FW, 0) < 200) pollw(IC<0>{}, IC<200>{});
                    waitv_barrier<after<X>(W3, 7, 200)>();
                } else if constexpr (N == MB) {
                    if constexpr (slot_of<X>(FW, 0) >= 200) pollw(IC<0>{}, IC<200 + N>{});
                    waitv_barrier<after<X>(HST, 3, 200 + N)>();
                    flag<1>(w, tag, srdws, WS_FH + (rt * 8 + ci) * 128);
                    STAMP(5);
                }
            });
            STAMP(6); STAMPX(6, 1);
            f32x16 pp = logits(acc2);
            PIN(pp);
            SB();
            ops(IC<300>{});
            prio<1>();
            const DualExp de(pp, rs1);
            u32x4 pq[4], zf[4], kf[4];
            STAMPX(6, 2);
            rep<0, 9>([&](auto I) {
                constexpr int i = I;
                if constexpr (i == XB) {
                    SB();
                    STAMPX(6, 3);
                    prio<0>();
                    f32x4 sm = {};
                    rep<0, 8>([&](auto J) {
                        waitv<after<X>(GA, J, 310 + J)>(gt[J]);
                        sm = MFMA16(asfrag(one), asfrag(gt[J]), sm);
                        PIN(sm);
                        ops(IC<310 + J>{});
                    });
                    PIN(sm);
                    STAMPX(6, 4);
                    auto again = [&](auto n, u32x4* d, auto load) {
                        for (int spin = 0; spin < (1 << 17); ++spin) {
                            u32 m = 0;
                            rep<0, n>([&](auto J) { load(J); });
                            rep<0, n>([&](auto J) { waitv<0>(d[J]); m = max(m, d[J][0]); });
                            if (__builtin_amdgcn_ballot_w64(m == ~0u) == 0) break;
                            __builtin_amdgcn_s_sleep(2);
                        }
                        f32x4 s = {};
                        rep<0, n>([&](auto J) { s = MFMA16(asfrag(one), asfrag(d[J]), s); });
                        return s;
                    };
                    if (w == ZWV) {
                        f32x4 sz = {};
#pragma unroll
                        for (int m = 0; m < 4; ++m) { PIN(of[m]); sz = MFMA16(asfrag(one), asfrag(of[m]), sz); }
                        if (__builtin_expect(__builtin_amdgcn_ballot_w64(sz[0] != sz[0]) != 0, 0))
                            sz = again(IC<4>{}, of, [&](auto M) { ld16c<M * 1024>(of[M], l16, srdws, WS_PZ + ci * 4096); });
                        L.w32(zw, pk2(add32(sz[0], sz[2]), add32(sz[1], sz[3])));
                    }
                    if (__builtin_expect(__builtin_amdgcn_ballot_w64(sm[0] != sm[0]) != 0, 0))
                        sm = again(IC<8>{}, gt, [&](auto J) { ld16c(gt[J], l16, srdws, sxor<J * 8192>(gb)); });
                    L.w64(kvw, u32x2{pk2(sm[0], sm[1]), pk2(sm[2], sm[3])});
                    ops(IC<318>{});
                    STAMPX(6, 5);
                    pollw(IC<1>{}, IC<320>{});
                    STAMPX(6, 6);
                    waitv_barrier<after<X>(CELL, 0, 2 * BF + 1, 320)>();
                    STAMP(7); STAMPX(6, 7);
                    prio<3>();
                    ops(IC<320>{});
#pragma unroll
                    for (int s = 0; s < 4; ++s) zf[s] = L.r128((s < 2 ? dz0 : dz1) + (s & 1) * 16);
#pragma unroll
                    for (int s = 0; s < 4; ++s) kf[s] = L.r128(fa + s * 256);
                    SB();
                }
                if constexpr (i < 8) {
                    const f32x2 ep = de.get(pp, i, false), en = de.get(pp, i, true);
                    u32 a = pk2(ep[0], ep[1]), c = pk2(en[0], en[1]);
                    PIN(a); PIN(c);
                    pq[i >> 2][i & 3] = a; pq[2 + (i >> 2)][i & 3] = c;
                    ops(IC<(i < XB ? 301 : 321) + i>{});
                    if constexpr (i & 1) SB();
                }
            });
            STAMPX(6, 8);
            {
                prio<2>();
                f32x4 dn = {};
#pragma unroll
                for (int s = 0; s < 4; ++s) dn = MFMA16(asfrag(zf[s]), asfrag(pq[s]), dn);
                PIN(dn);
                SB();
                ops(IC<330>{});
                f32x16 np = MFMA32(asfrag(kf[0]), asfrag(pq[0]), f32x16{});
                f32x16 nn = MFMA32(asfrag(kf[2]), asfrag(pq[2]), f32x16{});
                PIN(np); PIN(nn);
                SB();
                ops(IC<331>{});
                const float rd = __builtin_amdgcn_rcpf(fmaf(dn[3], dn[0], dn[2] * fmaf(dn[3], 1e-6f, dn[1])));
                np = MFMA32(asfrag(kf[1]), asfrag(pq[1]), np);
                nn = MFMA32(asfrag(kf[3]), asfrag(pq[3]), nn);
                PIN(np); PIN(nn);
                SB();
                ops(IC<332>{});
                float cp = dn[3] * rd; PIN(cp);
                float cn = dn[2] * rd; PIN(cn);
                STAMPX(6, 9);
                ops(IC<333>{});
                SB();
                np *= cp;
                PIN(np);
                ops(IC<334>{});
                prio<1>();
                SB();
                f32x16 o = __builtin_elementwise_fma(nn, f32x16{} + cn, np);
                PIN(o);
                ops(IC<335>{});
                SB();
                st16c(pack8(o, 0), os, srdws);
                st16c<256>(pack8(o, 1), os, srdws);
                SB();
            }
            STAMP(8); STAMPX(6, 10);
        }

        f32x4 as[4];
#pragma unroll
        for (int c = 0; c < 4; ++c) { if (c >= 2) am[0][c] = f32x4{}; am[1][c] = f32x4{}; as[c] = f32x4{}; }
        u32 bb[4], bc[4], xo, xi[3];
        s16x4 f[44][2];
        auto rdf = [&](int u, int j) {
            const int s = ustep(u), c = ucol(u);
            f[u][j] = L.trh((s < 4 ? ba[c] : (s == 8 || s == 9 ? bc[c] : bb[c])) + (s == 10 ? 0 : (s & 3) * 16384) + 512 * j);
        };
#pragma unroll
        for (int m = 0; m < 8; ++m) rdf(m >> 1, m & 1);
        rep<0, 88>([&](auto K) {
            constexpr int k = K, u = k < 76 ? k >> 1 : 38 + (k - 76) % 6, m = k < 76 ? k & 1 : k < 82, s = ustep(u), c = ucol(u), at = 400 + k;
            if constexpr (!(k & 7)) STAMPX(4, 1 + k / 8);
            if constexpr (k == (X ? 56 : 8 * BA)) pollw(IC<2>{}, IC<at>{});
            if constexpr (k && (k == 8 * B0 || k == 8 * BA || k == 56 || k == FK))
                waitv_barrier<cmin(k == FK ? after<X>(X ? HST : OST, 1, at) : 63, k == 8 * B0 ? after<X>(CELL, 0, 2 * BA + 1, at) : k == 8 * BA ? after<X>(CELL, 0, 15, at) :
                                   k == 56 ? cmin(after<X>(CELL, 16, 18, at), after<X>(LD, 20, 22, at)) : 63)>();
            if constexpr (k == 8 * BA) STAMP(9);
            if constexpr (k < 64 ? !(k & 7) : !SH && k < 70 && !(k & 1)) waitv<after<X>(HL, 2 * s + 1, at)>(hf[2 * s], hf[2 * s + 1]);
            if constexpr (SH && k == 64) {
                waitv<after<X>(HL, 21, at)>(hf[20], hf[21]);
                rep<16, 20>([&](auto J) { PIN(hf[J]); });
                settle(IC<6>{}, hf + 16, [&](auto J) { ld16u(hf[16 + J], hv[J & 1], srdws, ht[J >> 1]); });
            }
            SB();
            am[m][c] = MFMA16(Lds::join(f[u][0], f[u][1]), asfrag(hf[2 * s + m]), am[m][c]);
            PIN(am[m][c]);
            SB();
            constexpr int nb = B0 && k < 8 * B0 ? 8 * B0 : (k < 8 * BA ? 8 * BA : (k < 56 ? 56 : 88));
            if constexpr (k > 0 && (k == 8 * B0 || k == 8 * BA || k == 56)) prio<3>();
            else if constexpr (k == nb - 16 && (X || k > 8 * BF)) prio<2>();
            else if constexpr (k == nb - 8 && (X || k > 8 * BF)) prio<1>();
            else if constexpr (k == nb - 4) prio<0>();
            if constexpr (k < 64) rdf((k >> 1) + 4, k & 1);
            else {
#pragma unroll
                for (int hh = 0; hh < 16; ++hh) if (64 + hh * 3 / 4 == k) rdf(36 + hh / 2, hh & 1);
            }
            if constexpr (k == FK) flag<1>(w, tag, srdws, (X ? WS_FG : WS_FO) + (rt * 8 + ci) * 128);
            ops(IC<at>{});
            if constexpr (k >= 16 && k < 20) { bb[k - 16] = ba[k - 16] + 65536; PIN(bb[k - 16]); }
            if constexpr (k >= 24 && k < 28) { bc[k - 24] = ba[k - 24] + 131072; PIN(bc[k - 24]); }
            if constexpr (k >= 40 && k < 44) { ba[k - 40] -= (tg & 1) * 8192; PIN(ba[k - 40]); }
            if constexpr (k == 44) { outo = (l & 15) * 1024 + (l & 16) * 2; PIN(outo); }
            if constexpr (k == 46) { outo += ((l >> 1) & 16) + ((rb * 64 + 16 * tg) * 1024 + cb * 128 + 64 * hp); PIN(outo); }
            if constexpr (k == 48) { xo = L_XM + w2k + l16; PIN(xo); }
            if constexpr (k == 30) { ob = WS_O + prt * 131072 + hp * 65536 + (hb & 0xffff); PINS(ob); }
            if constexpr (k >= 52 && k < 55) { xi[k - 52] = xo ^ (k == 52 ? 8192 : (k == 53 ? 2048 : 10240)); PIN(xi[k - 52]); }
            if constexpr (k == 72 || k == 78) L.w128f(xo + (k == 78) * 1024, am[0][2 + (k == 78)]);
            if constexpr (k == 74 || k == 80) L.w128f(xo + 32768 + (k == 80) * 1024, am[1][2 + (k == 80)]);
            if constexpr (k == 84 || k == 86) L.w128f(xo + 16384 + (k == 86) * 1024, am[1][k == 86]);
        });
        SB();
        STAMP(10); STAMPX(4, 12); STAMPX(5, 1);
        ops(IC<488>{});
        STAMP(11); STAMPX(5, 3);
        waitv_barrier<after<X>(LD, 23, 30, 489)>();
        STAMPX(5, 4);
        prio<3>();
        f32x4 gate[2];
        bf16x8 wf[3][4];
        {
            f32x4 pm[2][3];
#pragma unroll
            for (int j = 0; j < 2; ++j)
#pragma unroll
                for (int k = 0; k < 3; ++k) pm[j][k] = L.r128f(xi[k] + k * 16384 + j * 1024);
            ops(IC<490>{});
            SB();
            rep<0, 2>([&](auto J) {
                constexpr int j = J;
#pragma unroll
                for (int c = 0; c < 4; ++c) wf[j][c] = L.tr(ba[c] + j * 8192, 512);
                ops(IC<491 + 2 * j>{});
                const f32x4 m = (am[0][j] + pm[j][0]) + (pm[j][1] + pm[j][2]), z = m * -LOG2E;
                const f32x4 d = f32x4{__builtin_amdgcn_exp2f(z[0]), __builtin_amdgcn_exp2f(z[1]), __builtin_amdgcn_exp2f(z[2]), __builtin_amdgcn_exp2f(z[3])} + 1.f;
                gate[j] = m * f32x4{__builtin_amdgcn_rcpf(d[0]), __builtin_amdgcn_rcpf(d[1]), __builtin_amdgcn_rcpf(d[2]), __builtin_amdgcn_rcpf(d[3])};
                PIN(gate[j]);
                ops(IC<492 + 2 * j>{});
                SB();
                prio<2 - j>();
            });
        }
        u32 xs = xo + (L_XS - L_XM);
        PIN(xs);
        f32x4 y0[2];
        u32 xres[4];
        rep<0, 8>([&](auto J) {
            constexpr int j = J;
            if constexpr (!(j & 1)) STAMPX(5, 5 + j / 2);
            if constexpr (!SO) waitv<after<X>(OL, j, 500 + j)>(of[j]);
            else if constexpr (j % OG == 0) {
                waitv<after<X>(OL, j + OG - 1, 500 + j)>(of[j + OG - 1]);
                rep<j, j + OG - 1>([&](auto J) { PIN(of[J]); });
                settle(IC<OG>{}, of + j, [&](auto J) { ld16u(of[j + J], hv[0], srdws, sxor<(j + J) * 8192>(ob)); });
            }
            rep<0, 4>([&](auto C) {
                constexpr int c = j == 7 ? (C + 2) & 3 : C;
                SB();
                as[c] = MFMA16(wf[j % 3][c], asfrag(of[j]), as[c]);
                PIN(as[c]);
                SB();
                if constexpr (C == 0) ops(IC<500 + j>{});
                if constexpr (C == 0 && j == 6) prio<0>();
                if constexpr (j < 6) wf[(j + 2) % 3][c] = L.tr(ba[c] + (j + 2) * 8192, 512);
                if constexpr (j == 7 && C == 0) {
                    waitv<after<X>(XR, 0, 510)>(xr);
                    const auto x0 = __builtin_amdgcn_permlane16_swap(xr[0], xr[2], false, false), x1 = __builtin_amdgcn_permlane16_swap(xr[1], xr[3], false, false);
                    xres[0] = x0[0]; xres[1] = x1[0]; xres[2] = x0[1]; xres[3] = x1[1];
                    PIN(xres[0]); PIN(xres[1]); PIN(xres[2]); PIN(xres[3]);
                }
                if constexpr (j == 7 && (C == 1 || C == 2)) { y0[C - 1] = unpack4(xres[2 * C - 2], xres[2 * C - 1]); PIN(y0[C - 1]); }
            });
        });
        STAMPX(5, 9);
        L.w128f(xs, as[2]);
        L.w128f(xs + 1024, as[3]);
        xs ^= 8192;
#pragma unroll
        for (int j = 0; j < 2; ++j) { y0[j] = __builtin_elementwise_fma(as[j], gate[j], y0[j]); PIN(y0[j]); }
        PIN(xs);
        STAMPX(5, 10);
        BARRIER();
        STAMP(12); STAMPX(5, 11);
        {
            u32 y[4];
#pragma unroll
            for (int j = 0; j < 2; ++j) {
                const f32x4 v = __builtin_elementwise_fma(L.r128f(xs + j * 1024), gate[j], y0[j]);
                y[2 * j] = pk2(v[0], v[1]); y[2 * j + 1] = pk2(v[2], v[3]);
            }
            const auto s0 = __builtin_amdgcn_permlane16_swap(y[0], y[2], false, false), s1 = __builtin_amdgcn_permlane16_swap(y[1], y[3], false, false);
            const auto ro = __builtin_amdgcn_make_buffer_rsrc((void*)((u64)kb[12] | ((u64)kb[13] << 32)), 0, 2048 * 1024, 0x00020000);
            __builtin_amdgcn_raw_buffer_store_b128(u32x4{s0[0], s1[0], s0[1], s1[1]}, ro, outo, 0, 0);
        }
    };
    if (b_ & 64) {
        body(std::true_type{});
        asm volatile("s_endpgm");
        UNDEFS(ka); UNDEFS(kb); UNDEFS(epoch); UNDEFS(b_); UNDEFS(w_); UNDEFS(w2k); UNDEFS(k0x); UNDEFS(srdx); UNDEFS(srdw); UNDEFS(srdw3); UNDEFS(rec);
        UNDEF(l_); UNDEF(vx0); UNDEF(vx1); UNDEF(vw0); UNDEF(vw1); UNDEF(selu); UNDEF(sidx);
#pragma unroll
        for (int i = 0; i < 4; ++i) { UNDEF(xa[i]); UNDEF(wa[i]); }
    }
    body(std::false_type{});
    asm volatile(".Lpf_end:");
#endif
}

extern "C" void kernel_launch(void* const* d_in, const int* in_sizes, int n_in, void* d_out, int out_size, void* d_ws, size_t ws_size,
                              hipStream_t stream) {
    auto two = [](u32 a, u32 b) { return (u64)a | ((u64)b << 32); };
#define D(p, bytes) p, two(bytes, 0x00020000u)
#define R(p, bytes, lds, first, step, row, extent, lane) p, two(bytes, lds), two(first, step), two((row) | ((extent) << 16), lane)
    hedgehog_mixer_kernel<<<256, 512, 0, stream>>>(
        D(d_in[0], 2048 * 1024), D(d_ws, WS_END), D(d_in[5], 512 * 2752), D(d_in[1], 512 * 3072), D(d_in[7], 1376 * 1024), D(d_in[3], 512 * 1024),
        D(d_in[2], 16384), D(d_out, 2048 * 1024),
        R(d_in[7], 1376 * 1024, L_DUMP, 0, 43 * 128, 1024, 43 * 128, 128), R(d_in[3], 512 * 1024, L_DUMP, 0, 2048, 1024, 2048, 128),
        R(d_in[1], 512 * 3072, L_DUMP, 0, 6144, 3072, 6144, 128), R(d_in[5], 512 * 2752, L_DUMP, 1728, 5504, 2752, 5504 - 1728, 128),
        R(d_in[6], 2752, L_B1, 0, 128, 32, 256, 4), R(d_in[8], 1024, L_B2, 0, 128, 32, 128, 4));
}
```
